```python
import torch

def _softmax(x, axis=-1):
    return torch.softmax(x, dim=axis)
def _gelu(x):
    return torch.nn.functional.gelu(x, approximate="tanh")
def _silu(x):
    return torch.nn.functional.silu(x)
def _sigmoid(x):
    return torch.sigmoid(x)
def _layernorm(x, axis=-1, eps=1e-5):
    m = x.mean(dim=axis, keepdim=True)
    v = x.var(dim=axis, keepdim=True, unbiased=False)
    return (x - m) / torch.sqrt(v + eps)
def _scatter_add(tgt, idx, src, axis=0):
    return tgt.clone().index_add_(axis, idx, src)
def _segment_sum(src, seg, n):
    out = torch.zeros((n,) + src.shape[1:], dtype=src.dtype)
    return out.index_add_(0, seg, src)
def _pad(x, pw):
    flat = tuple(v for pair in reversed(pw) for v in pair)
    return torch.nn.functional.pad(x, flat)

kernel_name = 'standalone__fft_conv__vision'

def setup_inputs():

    _w = (lambda a: (lambda t: t.to(torch.bfloat16) if t.is_floating_point() else t)(torch.as_tensor(a)))
    H = W = 224
    C = 96
    K = 7
    return {
        "x": _w((lambda *s: torch.randn(*s, dtype=torch.bfloat16) * 0.05)(1, H, W, C)),
        "w": _w((lambda *s: torch.randn(*s, dtype=torch.bfloat16) * 0.05)(K, K, C, C)),
    }

def reference(x, w):
    B, H, W, C = x.shape
    K = w.shape[0]
    wp = _pad(w, ((0, H - K), (0, W - K), (0, 0), (0, 0)))
    Xf = (lambda x, axes=(-2,-1): torch.fft.rfft2(x.float(), dim=axes))(x, axes=(1, 2))
    Wf = (lambda x, axes=(-2,-1): torch.fft.rfft2(x.float(), dim=axes))(wp, axes=(0, 1))
    Yf = torch.einsum('bhwi,hwio->bhwo', Xf, Wf)
    y = (lambda x, s, axes=(-2,-1): torch.fft.irfft2(x, s=s, dim=axes).to(torch.bfloat16))(Yf, (H, W), axes=(1, 2))
    return y
```

```cpp
#include <hip/hip_runtime.h>
#include <hip/hip_bf16.h>

typedef __bf16 bf16x8 __attribute__((ext_vector_type(8)));
typedef __bf16 bf16x2 __attribute__((ext_vector_type(2)));
typedef float  f32x4  __attribute__((ext_vector_type(4)));
typedef unsigned u32x2 __attribute__((ext_vector_type(2)));
typedef unsigned u32x4 __attribute__((ext_vector_type(4)));
typedef unsigned u32x6 __attribute__((ext_vector_type(6)));
typedef unsigned u32x8 __attribute__((ext_vector_type(8)));
typedef unsigned u32x10 __attribute__((ext_vector_type(10)));

#define LDS3 __attribute__((address_space(3)))

#define INSTR_PARAM
#define INSTR_ARG(k)
#define INSTR_DECL
#define STAMP(i)
#define CYC0()
#define CYC1()
#define STAMP_END(i)

constexpr int IMG = 224, CH = 96;
constexpr int NFW = 17;

constexpr int    XT = 224 * 64, XC = 8 * XT, XF = 2 * XC;
constexpr size_t XW_OFF = (size_t)36 << 20;
constexpr int    XC2 = 2 * XC, XF2 = 1 << 20;
constexpr size_t WT_OFF = 0;
constexpr int    WK = 16384, WP = 14 * WK, WH = 1 << 20, WF = 2 * WH;
constexpr size_t YH_OFF = (size_t)96 << 20;
constexpr int    YS = NFW * 1024, YG = 3 * 16384, YT = 3 * YG, YQ = 1 << 20;
constexpr size_t CN_OFF = (size_t)34 << 20;
constexpr int    CN_G = 64, CN_P = 56 * CN_G;
constexpr int    CN_BYTES = 2 * CN_P * 4;
constexpr size_t EP_OFF = (size_t)32 << 20 | 0xc0000;
constexpr unsigned long long EP_STEP = 0x9e3779b97f4a7c15ull;

struct CosTab { double v[448]; };
constexpr CosTab make_cos() {
  CosTab t{};
  const double pi = 3.14159265358979323846264338327950288;
  for (int k = 0; k <= 112; ++k) {
    const double a = 2.0 * pi * k / 448.0;
    double term = 1.0, sum = 1.0;
    for (int n = 1; n <= 14; ++n) { term *= -a * a / ((2.0 * n - 1.0) * (2.0 * n)); sum += term; }
    if (k == 112) sum = 0.0;
    t.v[k] = sum; t.v[(448 - k) % 448] = sum; t.v[224 - k] = -sum; t.v[224 + k] = -sum;
  }
  return t;
}
constexpr CosTab kCos = make_cos();
constexpr double cs(int idx) { return kCos.v[(idx + 448) % 448]; }
constexpr unsigned short bf16_rne(float f) {
  unsigned u = __builtin_bit_cast(unsigned, f);
  u += 0x7fffu + ((u >> 16) & 1u);
  return (unsigned short)(u >> 16);
}
constexpr float bf16_val(unsigned short b) { return __builtin_bit_cast(float, (unsigned)b << 16); }
constexpr unsigned short split_hl(double v, int hl) {
  const unsigned short hi = bf16_rne((float)v);
  return hl == 0 ? hi : bf16_rne((float)(v - (double)bf16_val(hi)));
}
struct FragD { unsigned short v[2][2][64][8]; };
struct TabM { unsigned short fh[2][2][2][64][8], fi[2][2][64][8], g[2][2][64][8]; FragD d; };
constexpr FragD make_D() {
  FragD t{};
  for (int ct = 0; ct < 2; ++ct) for (int l = 0; l < 64; ++l) for (int jj = 0; jj < 8; ++jj) {
    const int cp = 16 * ct + (l & 15), v = 8 * (l >> 4) + jj;
    const double e = cs(7 * (2 * (cp >> 1) + 1) * v + 112 * (cp & 1));
    for (int hl = 0; hl < 2; ++hl) t.v[ct][hl][l][jj] = split_hl(e, hl);
  }
  return t;
}
constexpr TabM make_M() {
  TabM t{};
  t.d = make_D();
  for (int s = 0; s < 2; ++s) for (int l = 0; l < 64; ++l) for (int jj = 0; jj < 8; ++jj) {
    const int n = l & 15, g = l >> 4, hh = jj >> 2, q = jj & 3;
    for (int ct = 0; ct < 2; ++ct) {
      const int kr = 16 * hh + 4 * g + q, a = kr >> 1, ri = kr & 1, cp = 4 * (4 * ct + (n & 3)) + (n >> 2), k = cp >> 1, ro = cp & 1;
      double e = 0.0;
      if (a < 14 && k < 14) e = (s == 0) ? cs(32 * k * a + 112 * (ro - ri)) : (ri == k / 7) ? cs(16 * (2 * (k % 7) + 1) * a + 112 * ro) : 0.0;
      for (int hl = 0; hl < 2; ++hl) t.fh[s][ct][hl][l][jj] = split_hl(e, hl);
    }
    const int k = 8 * hh + 2 * g + (q >> 1), ri = q & 1;
    {
      const int j = n >> 1, ro = n & 1;
      double e = 0.0;
      if (k < 14) e = (s == 0) ? cs(32 * k * (j + 6) + 112 * (ri - ro)) : (ro == k / 7) ? 2.0 * cs(16 * (2 * (k % 7) + 1) * (j + 6) + 112 * ri) : 0.0;
      for (int hl = 0; hl < 2; ++hl) t.fi[s][hl][l][jj] = split_hl(e, hl);
    }
    {
      const double e = (n < 14) ? cs(7 * (2 * k + 1) * (14 * s + n + 5) + 112 * ri) / 224.0 : 0.0;
      for (int hl = 0; hl < 2; ++hl) t.g[s][hl][l][jj] = split_hl(e, hl);
    }
  }
  return t;
}
__device__ __attribute__((aligned(4096))) char gXh[NFW * XF];
__device__ __attribute__((aligned(128))) unsigned gC[32];
__device__ __attribute__((aligned(64))) const TabM kTabM = make_M();

__device__ __forceinline__ unsigned pack2(float a, float b) {
  bf16x2 v; v[0] = (__bf16)a; v[1] = (__bf16)b;
  return __builtin_bit_cast(unsigned, v);
}
__device__ __forceinline__ f32x4 mfma32(u32x4 a, u32x4 b, f32x4 c) {
  return __builtin_amdgcn_mfma_f32_16x16x32_bf16(__builtin_bit_cast(bf16x8, a), __builtin_bit_cast(bf16x8, b), c, 0, 0, 0);
}
__device__ __forceinline__ u32x4 cat(u32x2 a, u32x2 b) { return u32x4{a[0], a[1], b[0], b[1]}; }
__device__ __forceinline__ u32x4 pack8(f32x4 a, f32x4 b) { return u32x4{pack2(a[0], a[1]), pack2(a[2], a[3]), pack2(b[0], b[1]), pack2(b[2], b[3])}; }
#define Z4 (f32x4{0.f, 0.f, 0.f, 0.f})
#define ST_AUX 16
typedef __amdgpu_buffer_rsrc_t rsrc_t;
__device__ __forceinline__ rsrc_t make_rsrc(void* p, unsigned bytes) { return __builtin_amdgcn_make_buffer_rsrc(p, 0, bytes, 0x00020000); }
constexpr unsigned OOB = 0x80000000u;
#define SB0() __builtin_amdgcn_sched_barrier(0)
#define BAR() __builtin_amdgcn_s_barrier()
#define VMW(n) asm volatile("s_waitcnt vmcnt(%0)" :: "i"(n) : "memory")
#define TRR(dst, addr, off) asm volatile("ds_read_b64_tr_b16 %0, %1 offset:%2" : "=v"(dst) : "v"(addr), "i"(off) : "memory")
#define GLD(dst, voff, sb, imm) asm volatile("global_load_dwordx4 %0, %1, %2 offset:%3" : "=v"(dst) : "v"(voff), "s"(sb), "i"(imm) : "memory")
#define DMA1(m0v, voff, sb, imm) asm volatile("s_mov_b32 m0, %0\n\ts_nop 0\n\tglobal_load_lds_dwordx4 %1, %2 offset:%3 sc1" :: "s"(m0v), "v"(voff), "s"(sb), "i"(imm) : "memory")
#define DMA(m0v, voff, sb, imm) asm volatile("s_mov_b32 m0, %0\n\ts_nop 0\n\tglobal_load_lds_dwordx4 %1, %2 offset:%3" :: "s"(m0v), "v"(voff), "s"(sb), "i"(imm) : "memory")
#define BST(v, voff, rs, so) asm volatile("buffer_store_dwordx4 %0, %1, %2, %3 offen sc1\n\ts_nop 1" :: "v"(v), "v"(voff), "s"(rs), "s"(so) : "memory")
#define WAIT2(n, a, b) asm volatile("s_waitcnt lgkmcnt(%2)" : "+v"(a), "+v"(b) : "i"(n) : "memory")
#define WAIT4(n, a, b, c, d) asm volatile("s_waitcnt lgkmcnt(%4)" : "+v"(a), "+v"(b), "+v"(c), "+v"(d) : "i"(n) : "memory")

#define WG_X 200
#define WG_W 200
constexpr int W_GATE = WG_X;
constexpr int W_SLOPE = 4, W_OFF = 290;
constexpr int F_RS = 768 * 16;
constexpr int F_CNT = 4 * F_RS;
constexpr int F_WOFF = F_CNT + 1024;
constexpr int F_LDS = F_WOFF + 56 * 512;
constexpr int F_NW0 = 32;
constexpr int F_NPF = 16;
constexpr int F_NWG = 256;

__device__ __forceinline__ int comp_of(int u, int n) { return n < 56 ? 56 * u + n : n < 60 ? 392 + 4 * u + n : 476; }
__device__ __forceinline__ void make_e(int cp, int g, u32x4 (&e)[2][2]) {
  const int fw = (cp * 2341) >> 16, rem = cp - 28 * fw, k = rem >> 1;
  const bool sp = fw >= 16;
  const float fa = sp ? 0.f : (float)(2 * fw + 1) * (1.f / 64.f);
  const float fb = (float)(sp ? 2 * (k >= 7 ? k - 7 : k) + 1 : 2 * k) * (1.f / 28.f);
#pragma unroll
  for (int ks = 0; ks < 2; ++ks) {
    const float f0 = fb * (float)(4 * ks + g) + ((rem & 1) ? 0.25f : 0.f);
    const bool ok = (ks == 0) || g < 3;
    const float m0 = (ok && (!sp || k >= 7)) ? 1.f : 0.f, m1 = (ok && !sp) ? 1.f : 0.f, m6 = (ok && (!sp || k < 7)) ? 1.f : 0.f;
    float v[8];
#pragma unroll
    for (int j = 0; j < 7; ++j) v[j] = __builtin_amdgcn_cosf(__builtin_fmaf(fa, (float)j, f0)) * (j == 0 ? m0 : j == 6 ? m6 : m1);
    v[7] = 0.f;
#pragma unroll
    for (int s = 0; s < 4; ++s) {
      const unsigned h = pack2(v[2 * s], v[2 * s + 1]);
      e[ks][0][s] = h;
      e[ks][1][s] = pack2(v[2 * s] - __builtin_bit_cast(float, h << 16), v[2 * s + 1] - __builtin_bit_cast(float, h & 0xffff0000u));
    }
  }
}

#define GATE(lim, slope, off) do { unsigned lim_; \
    asm volatile("s_memrealtime vcc\n\ts_waitcnt lgkmcnt(0)\n\ts_sub_u32 vcc_lo, vcc_lo, %1\n\ts_mul_i32 %0, vcc_lo, %3\n\ts_sub_i32 %0, %0, %4\n\ts_max_i32 %0, %0, 0\n\ts_min_u32 %0, %0, %2\n" \
                 "1:\n\ts_cmp_ge_u32 vcc_lo, %0\n\ts_cbranch_scc1 2f\n\ts_memrealtime vcc\n\ts_waitcnt lgkmcnt(0)\n\ts_sub_u32 vcc_lo, vcc_lo, %1\n\ts_branch 1b\n2:" \
                 : "=&s"(lim_) : "s"((unsigned)t0), "i"(lim), "i"(slope), "i"(off) : "memory", "vcc", "scc"); } while (0)
#define KPTR(i) ((char*)(((unsigned long long)ka[2 * (i) + 1] << 32) | ka[2 * (i)]))
#define WSP(i) ((char*)(((unsigned long long)KPTR(i) + 0x3fffffu) & ~0x3fffffull))
#define PLD() asm volatile("global_load_dwordx2 %0, %1, %2 offset:128 sc1" : "=v"(pw) : "v"(0u), "s"(ep) : "memory")
#define PLC(n) do { unsigned st_; asm volatile("s_mov_b32 %[sp], 0\n\ts_waitcnt vmcnt(%[nn])\n" \
    "1:\n\tv_lshrrev_b64 %[t], 4, %[v]\n\tv_cmp_ne_u64 vcc, %[tg], %[t]\n\ts_nop 4\n\ts_cbranch_vccz 2f\n\t" \
    "s_add_u32 %[sp], %[sp], 1\n\ts_cmp_ge_u32 %[sp], %[lim]\n\ts_cbranch_scc1 2f\n\ts_sleep 2\n\t" \
    "global_load_dwordx2 %[v], %[z], %[p] offset:128 sc1\n\ts_waitcnt vmcnt(0)\n\ts_branch 1b\n2:" \
    : [v] "+v"(pw), [sp] "=&s"(st_), [t] "=&v"(pt) : [z] "v"(0u), [p] "s"(ep), [tg] "s"(tag >> 4), [lim] "i"(SPIN_MAX), [nn] "i"(n) : "memory", "scc", "vcc"); \
    pl = __builtin_amdgcn_readfirstlane(pw[0]); } while (0)
#define PMASK(pl_) ((0xf0f0u >> ((pl_) & 7)) ^ (0u - (((pl_) >> 3) & 1)))
constexpr int SPIN_MAX = 1 << 16;
constexpr int F_LEAD = 24;
#define LANDED(sh) do { unsigned n_; \
    asm volatile("s_waitcnt vmcnt(0)\n\ts_mov_b64 exec, %3\n\tds_add_u32 %1, %2\n\ts_mov_b64 exec, -1\n" \
                 "1:\n\tds_read_u8 %0, %1 offset:%4\n\ts_waitcnt lgkmcnt(0)\n\tv_cmp_gt_u32 vcc, 4, %0\n\ts_nop 4\n\ts_cbranch_vccnz 1b" \
                 : "=&v"(n_) : "v"(sbase + F_CNT), "v"(1u << (8 * (sh))), "s"((sh) ? (unsigned long long)(wave >> 2) : 1ull), "i"(sh) : "memory", "vcc"); SB0(); } while (0)
#define WDMA() do { if (wave >= 4) { const char* wgt = KPTR(1) + oW; \
    _Pragma("unroll") for (int i = 0; i < 7; ++i) DMA(sbase + F_WOFF + w4 * 1024 + i * 4096, wv[i & 1], wgt + i * (7 * CH * CH * 2), 0); } } while (0)
#define KWAIT() do { SB0(); asm volatile("s_waitcnt lgkmcnt(0)\n\ts_memrealtime %0" : "=s"(t0) :: "memory"); SB0(); asm volatile("" : "+s"(ka)); } while (0)

extern "C" __global__ __launch_bounds__(512, 2)
void k_fwd(const char* __restrict__ x_, const char* __restrict__ wgt_, char* __restrict__ ws_, char* __restrict__ out_ INSTR_PARAM)
{
  __shared__ __attribute__((aligned(16))) char smem[F_LDS];
  INSTR_DECL;
  u32x8 ka;
  unsigned long long cb, tag, pt, t0;
  u32x2 pw;
  unsigned pl, tf;
  const unsigned sbase = (unsigned)(size_t)(LDS3 char*)smem;
  const int b = blockIdx.x;
  {
    unsigned s0, s1, s2, va, vb, vc, vd, ve, vf, vg;
    unsigned long long vz;
    asm volatile("s_load_dwordx8 %[ka], %[kp], 0\n\tv_readfirstlane_b32 %[tf], %[id]\n\t"
                 "v_mov_b64 %[vz], 0\n\tv_mov_b32 %[va], %[sb]\n\tds_write_b64 %[va], %[vz] offset:%[co]\n\t"
                 "s_cmp_lt_u32 %[b], 32\n\ts_cbranch_scc1 1f\n\t"
                 "s_and_b32 %[s0], %[b], 7\n\ts_mul_i32 %[s0], %[s0], 28\n\ts_lshr_b32 %[s1], %[b], 3\n\ts_add_i32 %[s0], %[s0], %[s1]\n\ts_add_i32 %[s0], %[s0], -4\n\t"
                 "v_and_b32 %[va], 63, %[id]\n\t"
                 "s_and_b32 %[s1], %[s0], -16\n\ts_lshr_b32 %[s2], %[s0], 1\n\ts_and_b32 %[s2], %[s2], 6\n\ts_add_i32 %[s1], %[s1], %[s2]\n\t"
                 "v_cmp_lt_u32 vcc, 31, %[va]\n\tv_lshlrev_b32 %[va], 4, %[va]\n\t"
                 "s_lshr_b32 %[s2], %[tf], 6\n\ts_mul_i32 %[s2], %[s2], 5\n\ts_and_b32 %[s2], %[s2], 9\n\ts_add_i32 %[s1], %[s1], %[s2]\n\t"
                 "v_xor_b32 %[vc], 32, %[va]\n\t"
                 "s_add_i32 %[s2], %[s1], -6\n\ts_addk_i32 %[s1], 218\n\ts_min_u32 %[s1], %[s1], %[s2]\n\t"
                 "s_and_b32 %[s0], %[s0], 3\n\ts_cselect_b32 %[s2], 0, 43008\n\t"
                 "v_cndmask_b32 %[vb], %[va], %[vc], vcc\n\tv_add_u32 %[vd], %[s2], %[va]\n\t"
                 "s_lshl2_add_u32 %[s1], %[s1], %[s0]\n\ts_mulk_i32 %[s1], 10752\n\ts_addk_i32 %[s1], 2560\n\t"
                 "v_cndmask_b32 %[ve], %[vd], %[vc], vcc\n\t"
                 "s_lshl_b32 %[s2], %[b], 3\n\ts_and_b32 %[s2], %[s2], 0xffffffc0\n\ts_addk_i32 %[s2], -256\n\t"
                 "v_lshrrev_b32 %[vf], 3, %[id]\n\tv_add_u32 %[vf], %[s2], %[vf]\n\tv_min_u32 %[vf], 1763, %[vf]\n\t"
                 "v_mul_u32_u24 %[vg], 10923, %[vf]\n\tv_lshrrev_b32 %[vg], 16, %[vg]\n\tv_mul_u32_u24 %[vg], 18, %[vg]\n\t"
                 "s_and_b32 %[s2], %[b], 3\n\ts_mul_i32 %[s2], %[s2], 6\n\tv_add3_u32 %[vf], %[vf], %[vg], %[s2]\n\tv_lshlrev_b32 %[vf], 7, %[vf]\n\t"
                 "s_add_i32 m0, %[sb], %[co] + 256\n\ts_mul_i32 %[s0], %[tf], 192\n\t"
                 "s_waitcnt lgkmcnt(0)\n\tglobal_load_lds_dword %[vf], s[14:15]\n\t"
                 "s_add_u32 vcc_lo, s16, 0x3fffff\n\ts_addc_u32 vcc_hi, s17, 0\n\ts_and_b32 vcc_lo, vcc_lo, 0xffc00000\n\ts_mov_b32 %[s2], %[eo]\n\t"
                 "s_load_dwordx2 %[tg], vcc, %[s2]\n\ts_add_i32 m0, %[s0], %[sb]\n\t"
                 "s_barrier\n\ts_cmpk_ge_u32 %[tf], 0x100\n\ts_cbranch_scc1 1f\n\ts_setprio 3\n\ts_memrealtime %[t0]\n\t"
                 "s_mov_b64 vcc, s[12:13]\n\ts_add_u32 vcc_lo, vcc_lo, %[s1]\n\ts_addc_u32 vcc_hi, vcc_hi, 0\n\t"
                 "global_load_lds_dwordx4 %[vd], vcc offset:-4096\n\tglobal_load_lds_dwordx4 %[ve], vcc offset:-3072\n\t"
                 "global_load_lds_dwordx4 %[vc], vcc offset:-2048\n\tglobal_load_lds_dwordx4 %[va], vcc offset:-1024\n\t"
                 "global_load_lds_dwordx4 %[vb], vcc\n\tglobal_load_lds_dwordx4 %[vc], vcc offset:1024\n\t"
                 "global_load_lds_dwordx4 %[va], vcc offset:2048\n\tglobal_load_lds_dwordx4 %[vb], vcc offset:3072\n\t"
                 "s_addk_i32 m0, 0x1000\n\ts_add_u32 vcc_lo, vcc_lo, 0x1000\n\ts_addc_u32 vcc_hi, vcc_hi, 0\n\t"
                 "global_load_lds_dwordx4 %[vc], vcc\n\tglobal_load_lds_dwordx4 %[va], vcc offset:1024\n\t"
                 "global_load_lds_dwordx4 %[vb], vcc offset:2048\n\tglobal_load_lds_dwordx4 %[vc], vcc offset:3072\n\t"
                 "s_waitcnt lgkmcnt(0)\n1:"
                 : [ka] "=&{s[12:19]}"(ka), [tf] "=&s"(tf), [t0] "=&s"(t0), [s0] "=&s"(s0), [s1] "=&s"(s1), [s2] "=&s"(s2),
                   [va] "=&v"(va), [vb] "=&v"(vb), [vc] "=&v"(vc), [vd] "=&v"(vd), [ve] "=&v"(ve), [vz] "=&v"(vz), [vf] "=&v"(vf), [vg] "=&v"(vg), [tg] "=&s"(tag)
                 : [kp] "s"(__builtin_amdgcn_kernarg_segment_ptr()), [id] "v"(threadIdx.x), [b] "s"(b), [sb] "s"(sbase + 4096u), [co] "i"(F_CNT - 4096), [eo] "i"((unsigned)EP_OFF)
                 : "memory", "scc", "vcc");
  }
  const int lane = threadIdx.x & 63;
  const int wave = tf >> 6, w4 = wave & 3;
  const int g = lane >> 4, q = (lane >> 2) & 3, p = lane & 3, c = lane & 15;
  if (__builtin_expect(wave < 4 && b >= F_NW0, 0)) {
    STAMP(0);
    u32x4 tw[2];
    {
      const unsigned to = ((threadIdx.x & 64u) << 5) | (lane << 4);
      GLD(tw[0], to, (const char*)&kTabM + 16384, 0); GLD(tw[1], to, (const char*)&kTabM + 16384, 1024);
    }
    char* ws = WSP(2);
    const char* ep = ws + EP_OFF;

    SB0();
    const int L = (b & 7) * 28 + ((b - F_NW0) >> 3);
    const int tp = L & 3;
    const int tl = wave >> 1, ct = wave & 1;
    unsigned tb[2][2], ts[2];
#pragma unroll
    for (int hh = 0; hh < 2; ++hh) {
      const unsigned px = 28 * tl + 3 + 8 * g + 4 * hh + q, base = sbase + px * 192 + p * 8, gb = (px & 8) << 2;
      tb[0][hh] = base + gb; tb[1][hh] = base - gb;
      const unsigned ps = 28 * hh + 2 + 32 * (q & 1) + (q >> 1);
      ts[hh] = sbase + g * F_RS + ps * 192 + ((wave * 32) ^ ((ps & 8) << 2)) + p * 8;
    }
        const u32x4 rs = {(unsigned)(size_t)gXh, (unsigned)((size_t)gXh >> 32) & 0xffffu, NFW * XF, 0x00020000u};
    const unsigned vin = (unsigned)(2 * tp * XT + (L >> 2) * 256) + c * 16;
    const unsigned vo = (unsigned)((8 * ct + 2 * g) * XF + tl * XT) + vin;
    const unsigned long long xw = (unsigned long long)(ws + XW_OFF);
    const u32x4 rs2 = {(unsigned)xw, (unsigned)(xw >> 32) & 0xffffu, NFW * XF2, 0x00020000u};
    const unsigned vi2 = vin + tl * XT, vs1 = g * XT + vin;
    unsigned vo2 = (unsigned)(8 * ct + 2 * g) * XF2 + ((vi2 >> 13) << 14) + (vi2 & 8191);
    const unsigned vs = (g < 2) ? vs1 : OOB, vs2 = (g < 2) ? ((vs1 >> 13) << 14) + (vs1 & 8191) : OOB;
    u32x4 sel[2];
#pragma unroll
    for (int i = 0; i < 8; ++i) sel[i >> 2][i & 3] = ((g >> 1) == (i >> 2) && c == 4 * ((i >> 1) & 1) + 2 * (g & 1) + (i & 1)) ? 0x3f803f80u : 0u;
    {
      LANDED(0);
      asm volatile("" : "+v"(tw[0]), "+v"(tw[1]));
      tag += EP_STEP;
    }
    STAMP(1);
    PLD();
    u32x2 da[2][8];
    f32x4 acc[2][4];
    u32x4 o;
#define F_RD(it, k_) TRR(da[(it) & 1][k_], tb[(it) & 1][(k_) & 1], (it) * 32 + ((k_) >> 1) * F_RS)
#define F_EPI(it, m) do { const f32x4 (&e_)[4] = acc[(it) & 1]; const int s_ = ((m) >> 2) * 2; \
      if (((m) & 3) == 1) { o[0] = pack2(e_[0][s_], e_[0][s_ + 1]); o[1] = pack2(e_[1][s_], e_[1][s_ + 1]); } \
      if (((m) & 3) == 2) { o[2] = pack2(e_[2][s_], e_[2][s_ + 1]); o[3] = pack2(e_[3][s_], e_[3][s_ + 1]); } \
      if (((m) & 3) == 3) { if ((it) < 2) BST(o, vo, rs, (it) * XC + ((m) >> 2) * XF); else BST(o, vo2, rs2, ((it) - 2) * XC2 + ((m) >> 2) * XF2); } } while (0)
#pragma unroll
    for (int k = 0; k < 8; ++k) F_RD(0, k);
#pragma unroll
    for (int it = 0; it < 6; ++it) {
      if (it == 1) { STAMP(3); GATE(W_GATE, W_SLOPE, W_OFF); STAMP(2); __builtin_amdgcn_s_setprio(0); SB0(); }
      if (it == 3) { PLC(4); vo2 |= ((((PMASK(pl) ^ 0xaau) >> (4 * ct + g)) & 1u) << 13); STAMP(7); SB0(); }
#pragma unroll
      for (int m = 0; m < 8; ++m) {
        u32x2 (&d_)[8] = da[it & 1];
        if ((m & 1) == 0) WAIT2((it < 5 || m < 4) ? 6 : 10 - m, d_[m], d_[m + 1]);
        f32x4& a_ = acc[it & 1][m >> 1];
        a_ = mfma32(tw[(m & 1) ^ 1], cat(d_[m & 6], d_[m | 1]), (m & 1) ? a_ : Z4);
        SB0();
        if (it < 5) F_RD(it + 1, m);
        else if (m < 4) TRR(da[0][m], ts[m & 1], (m >> 1) * 128);
        if (it > 0) F_EPI(it - 1, m);
        SB0();
      }
    }
    WAIT4(0, da[0][0], da[0][1], da[0][2], da[0][3]);
#pragma unroll
    for (int m = 0; m < 4; ++m) acc[0][m] = mfma32(sel[m & 1], cat(da[0][m & 2], da[0][m | 1]), Z4);
    SB0();
#pragma unroll
    for (int m = 0; m < 8; ++m) F_EPI(5, m);
    if (wave < 2) {
      BST(pack8(acc[0][0], acc[0][1]), vs, rs, 16 * XF + wave * XC);
      BST(pack8(acc[0][2], acc[0][3]), vs2, rs2, 16 * XF2 + (wave + 2) * XC2);
    } else BST(pack8(acc[0][0], acc[0][1]), vs2, rs2, 16 * XF2 + (wave - 2) * XC2);
  } else {
    __builtin_amdgcn_s_setprio(1);
    unsigned pf[2];
    {
      unsigned cl;
      asm volatile("s_cmp_ge_u32 %4, %5\n\ts_cbranch_scc1 1f\n\t"
                   "s_getpc_b64 %3\nfftconv_code_pc:\n\t"
                   "s_mov_b32 %0, fftconv_code_end-fftconv_code_pc\n\t"
                   "v_lshlrev_b32 %2, 6, %6\n\tv_min_u32 %1, %0, %2\n\tv_min_u32 %2, 0x4fc0, %2\n\t"
                   "global_load_dword %1, %1, %3\n\tglobal_load_dword %2, %2, %7\n1:"
                   : "=&s"(cl), "=&v"(pf[0]), "=&v"(pf[1]), "=&s"(cb)
                   : "s"(b), "i"(F_NPF), "v"(threadIdx.x), "s"(&kTabM) : "memory", "scc");
    }
    const int bj = min(b >> 4, (b >> 5) + 1), cc = (bj * 11) >> 5, wp = bj - 3 * cc, ioc = 4 * cc + (b & 3);
    const int wi = b >= F_NW0 ? b >> 3 : 2 * (b >> 3) + (wave < 4 ? 1 : 0);
    unsigned wv[2];
#pragma unroll
    for (int i = 0; i < 2; ++i) {
      const unsigned hi = lane >> 5, j = ((lane & 31) >> 1) ^ (4 * i + ((2 * w4 + hi) & 3));
      wv[i] = (w4 < 3 ? hi : 0) * (CH * CH * 2) + ((j >> 2) + 8 * (j & 2)) * (CH * 2) + (j & 1) * 32 + (lane & 1) * 16;
    }
    unsigned oW = (unsigned)((32 * (ioc >> 2) + 4 * (ioc & 3)) * (CH * 2) + 64 * wp + 2 * w4 * (CH * CH * 2));
    asm volatile("" : "+v"(wv[0]), "+v"(wv[1]));
    unsigned xcc;
    asm volatile("s_getreg_b32 %0, hwreg(HW_REG_XCC_ID)" : "=s"(xcc));
    unsigned rot = xcc - b;
    asm volatile("" : "+s"(oW));
    KWAIT();
    STAMP(0);
    char* ws = WSP(2);
    const char* ep = ws + EP_OFF;
    asm volatile("s_cmp_ge_u32 %2, 32\n\ts_cbranch_scc1 1f\n\ts_load_dwordx2 %0, %1, 0\n\ts_barrier\n1:" : "+s"(tag) : "s"(ep), "s"(b) : "memory", "scc");
    if ((b >> 3) == F_LEAD / 8) {
      if (wave == 0) {
        unsigned long long t1, t2, old;
        const unsigned a = lane * 16 + (b & 7) * 128;
        STAMP(11);
        asm volatile("s_load_dwordx2 %2, %6, 128\n\ts_mov_b64 exec, 0xff\n\t.p2align 6\n\t"
                     "global_store_dwordx4 %3, %5, %6 offset:256 sc1\n\tglobal_store_dwordx4 %4, %5, %6 offset:256 sc1\n\t"
                     "s_waitcnt vmcnt(1)\n\ts_memtime %0\n\ts_waitcnt vmcnt(0)\n\ts_memtime %1\n\ts_mov_b64 exec, -1\n\ts_waitcnt lgkmcnt(0)"
                     : "=&s"(t1), "=&s"(t2), "=&s"(old) : "v"(a), "v"(a + 8192u), "v"(u32x4{0u, 0u, 0u, 0u}), "s"(ep) : "memory");
        SB0();
        unsigned long long tg = tag;
        asm volatile("" : "+s"(tg));
        tg += EP_STEP;
        STAMP(12);
        const unsigned g0 = (unsigned)(t2 - t1);
        const unsigned v = ((unsigned)tg & ~15u) | (rot & 7u) | (((g0 > 48u ? 0u : 8u) ^ (xcc * 2)) & 8u);
        asm volatile("s_xor_b64 vcc, %3, %4\n\ts_lshr_b64 vcc, vcc, 4\n\ts_cselect_b64 exec, 1, 0\n\t"
                     "global_atomic_cmpswap_x2 %0, %1, %2 offset:128 sc1\n\ts_nop 1\n\ts_mov_b64 exec, -1"
                     :: "v"(0u), "v"(u32x4{v, (unsigned)(tg >> 32), (unsigned)old, (unsigned)(old >> 32)}), "s"(ep), "s"(old), "s"(tg) : "memory", "vcc", "scc");
        STAMP(6);
      }
      BAR();
    }
    if (b < F_NW0) WDMA();
    STAMP(10);
    SB0();
    u32x4 e[2][2], wq[2][4];
    unsigned ta[2][8];
#pragma unroll
    for (int j = 0; j < 8; ++j) {
      ta[0][j] = sbase + F_WOFF + (8 * g + q) * 512 + ((j ^ (4 * (g & 1) + q)) << 5) + p * 8;
      ta[1][j] = ta[0][j] + (g == 3 ? 8192 : 16384);
      asm volatile("" : "+v"(ta[0][j]), "+v"(ta[1][j]));
    }
#define W_UE() do { u = (b - ((b + rot) & 3) + (wi & 3)) & 7; make_e(comp_of(u, 16 * w4 + c), g, e); \
      asm volatile("" : "+v"(e[0][0]), "+v"(e[0][1]), "+v"(e[1][0]), "+v"(e[1][1])); } while (0)
    unsigned vo;
    int u;
    W_UE();
    SB0();
    STAMP(1);
    if (b >= F_NW0) WDMA();
    LANDED(1);
    asm volatile("" : "+v"(pf[0]), "+v"(pf[1]), "+s"(t0), "+s"(tag));
    tag += EP_STEP;
    PLD();
    STAMP(9);
   for (bool first = true;;) {
#define W_ST(k) __builtin_amdgcn_raw_buffer_store_b128(wq[(k) >> 2][(k) & 3], rsw, vo + ((k) & 1) * 1024, so + ((k) >> 2) * WH + (((k) >> 1) & 1) * WK, ST_AUX)
    u32x2 d[3][4];
    f32x4 acc[4];
#define W_RD(n, k_) TRR(d[(n) % 3][k_], ta[(k_) >> 1][(2 * (n) + ((n) >> 3)) & 7], (((n) >> 2) & 1) * 256 + ((k_) & 1) * 2048)
#pragma unroll
    for (int k = 0; k < 8; ++k) W_RD(k >> 2, k & 3);
#pragma unroll
    for (int n = 0; n < 16; ++n) {
      u32x2 (&d_)[4] = d[n % 3];
      WAIT4((n < 15) ? 4 : 0, d_[0], d_[1], d_[2], d_[3]);
#pragma unroll
      for (int m = 0; m < 4; ++m) {
        acc[n & 3] = mfma32(e[m & 1][(m >> 1) ^ 1], cat(d_[2 * (m & 1)], d_[2 * (m & 1) + 1]), m ? acc[n & 3] : Z4);
        SB0();
        if (n < 14) W_RD(n + 2, m);
        if (n > 2 && (n & 1)) wq[(n - 3) >> 3][m][((n - 3) >> 1) & 3] = pack2(acc[(n - 3) & 3][m], acc[(n - 2) & 3][m]);
        SB0();
      }
    }
#pragma unroll
    for (int r = 0; r < 4; ++r) wq[1][r][3] = pack2(acc[2][r], acc[3][r]);
    asm volatile("" : "+v"(wq[0][0]), "+v"(wq[0][1]), "+v"(wq[0][2]), "+v"(wq[0][3]), "+v"(wq[1][0]), "+v"(wq[1][1]), "+v"(wq[1][2]), "+v"(wq[1][3]));
    STAMP(3);
    if (!first) break;
    first = false;
    PLC(0);
    if (__builtin_expect(((pl ^ rot) & 3u) == 0, 1)) break;
    rot = pl;
    W_UE();
   }
    {
      const int cp0 = comp_of(u, 16 * w4 + 4 * g), fw = (cp0 * 2341) >> 16;
      vo = (unsigned)(fw * WF + (cp0 - 28 * fw) * (WK / 2) + (ioc & 3) * 256 + c * 16) | (fw < 16 ? (((PMASK(pl) >> u) ^ fw) & 1u) << 13 : 0u);
    }
    const rsrc_t rsw = make_rsrc(ws + WT_OFF, NFW * WF);
    const int so = wp * WP + (ioc >> 2) * 2048;
    asm volatile("" : "+v"(vo));
    STAMP(7);
    GATE(WG_W, 64, 0);
    STAMP(2);
    if (b < 4)
      __builtin_amdgcn_raw_buffer_store_b128(u32x4{0u, 0u, 0u, 0u}, make_rsrc(ws + CN_OFF, CN_BYTES), b * 8192 + threadIdx.x * 16, 0, ST_AUX);
    if (b == 4 && wave == 0) asm volatile("global_store_dword %0, %1, %2 sc1" :: "v"((lane & 7) * 4), "v"(PMASK(pl)), "s"(gC) : "memory");
#pragma unroll
    for (int k = 0; k < 8; ++k) W_ST(k);
  }
  STAMP(4);
  STAMP_END(5);
}

constexpr int M_XT = 1824;
constexpr int M_RA = 0, M_RB = 32 * M_XT;
constexpr int M_ZT = 912, M_Z1 = 32 * M_ZT;
constexpr int M_TAB = M_RB + 3 * M_Z1;
constexpr int M_LDS = M_TAB + 8192;
constexpr int M_NWG = 240, M_NI = NFW * 14, I_NWG = 224;
constexpr int POLL_SLEEP = 2;
#define H_LO 0

#define DSR(dst, addr, off) asm volatile("ds_read_b128 %0, %1 offset:%2" : "=v"(dst) : "v"(addr), "i"(off) : "memory")
#define DSW(addr, v, off) asm volatile("ds_write_b128 %0, %1 offset:%2" :: "v"(addr), "v"(v), "i"(off) : "memory")
#define WLD GLD
#define XC_(r) "+v"(r)
#define PIN(r) asm volatile("" :: "v"(r))
#define MM(d, a, b, c) do { d = mfma32(a, b, c); PIN(d); } while (0)
#define WAITV(n, a, b) asm volatile("s_waitcnt vmcnt(%2)" : "+v"(a), "+v"(b) : "i"(n) : "memory")
#define LGW0() asm volatile("s_waitcnt lgkmcnt(0)" ::: "memory")
#define XLD(cc, chg, i) do { if ((i) < 2) GLD(xr[cc][chg][(i) & 1], vL, (cc) ? xs2 + (2 * (cc) + (chg) - 2) * XC2 : xs + (chg) * XC, (i) * 1024); \
    else asm volatile("global_load_dwordx2 %0, %1, %2" : "=v"(xh[cc][chg]) : "v"(vL), "s"((cc) ? xsh2 + (2 * (cc) + (chg) - 2) * XC2 : xsh + (chg) * XC) : "memory"); } while (0)
#define ARRIVE(p) asm volatile("s_mov_b64 exec, 1\n\tglobal_store_dword %0, %1, %2 sc1\n\ts_mov_b64 exec, -1" :: "v"(0u), "v"(1u), "s"(p) : "memory")
#define YST(v, sb) asm volatile("global_store_dwordx4 %0, %1, %2 sc1\n\ts_nop 1" :: "v"(yv), "v"(v), "s"(sb) : "memory")

extern "C" __global__ __launch_bounds__(512, 2)
void k_mix(char* __restrict__ ws_, char* __restrict__ out_ INSTR_PARAM)
{
  __shared__ __attribute__((aligned(16))) char smem[M_LDS];
  u32x4 ka;
  asm volatile("s_load_dwordx4 %0, %1, 0" : "=s"(ka) : "s"(__builtin_amdgcn_kernarg_segment_ptr()) : "memory");
  unsigned pm, cbit;
  asm volatile("s_load_dword %0, %1, 0" : "=s"(pm) : "s"(gC) : "memory");
  int lane = threadIdx.x & 63, g, r, q, p;
  const int wave = __builtin_amdgcn_readfirstlane(threadIdx.x >> 6);
  const unsigned sbase = (unsigned)(size_t)(LDS3 char*)smem;
  const int slot = blockIdx.x & 7, lj = blockIdx.x >> 3;
  const int L = (lj < 28 ? 28 * slot : 196 + 2 * slot) + lj;
  if (L >= M_NI) {
    asm volatile("s_waitcnt lgkmcnt(0)" : "+s"(ka), "+s"(pm));
    if (L == M_NI && threadIdx.x == 0) {
      unsigned long long* e = (unsigned long long*)(WSP(0) + EP_OFF);
      __hip_atomic_store(e, *e + EP_STEP, __ATOMIC_RELAXED, __HIP_MEMORY_SCOPE_AGENT);
    }
    return;
  }
  const int fw = L / 14, G = (L - 14 * fw) >> 1, half = L & 1;
  unsigned vL = lane * 16;
  const char* tabp = (const char*)&kTabM;
  u32x4 fh[2][2], fi[2], gi[2][2];
  const int Q = 8 * G + wave;
  unsigned oX = (unsigned)(fw * XF + Q * 2048);
  int k0, k1, mw;
  unsigned xl0;
  INSTR_DECL; STAMP(0);
  const char *xs = gXh + oX, *xsh, *xs2, *xsh2, *ws[2];
  char* w_;
#define XPIN() asm volatile("" : "+s"(xs))
  char *ys, *Yh;
  unsigned *cnt, *flp;
  u32x4 xr[3][2][2];
  u32x2 xh[3][2];
#define TABF() do { int l_ = L; asm volatile("" : "+s"(l_)); const char* tf = tabp + (l_ >= 224 ? 4096 : 0); xsh = xs + (Q - 7 * ((Q * 37) >> 8) == 6 ? 2048 - XT : 2048); \
    GLD(fh[0][0], vL, tf, 0); GLD(fh[1][0], vL, tf, 2048); if (H_LO) { GLD(fh[0][1], vL, tf, 1024); GLD(fh[1][1], vL, tf, 3072); } } while (0)
#define SETUP_X() do { \
    const int qh_ = Q - 7 * ((Q * 37) >> 8) == 6 ? Q - 6 : Q + 1; \
    unsigned o_ = (unsigned)XW_OFF + fw * XF2 + ((fw >> 1) & 1) * 8192; \
    unsigned o1_ = o_ + (Q >> 2) * 16384 + (Q & 3) * 2048, o2_ = o_ + (qh_ >> 2) * 16384 + (qh_ & 3) * 2048; \
    asm volatile("" : "+s"(o1_), "+s"(o2_)); \
    LGW0(); SB0(); asm volatile("" : "+s"(ka), "+s"(pm)); \
    w_ = WSP(0); \
    cbit = fw < 16 ? ((pm >> slot) & 1u) << 13 : 0u; \
    xs2 = w_ + (o1_ ^ cbit); xsh2 = w_ + (o2_ ^ cbit); \
    asm volatile("" : "+s"(w_), "+s"(xs2), "+s"(xsh2)); \
  } while (0)
#define SETUP_P() do { \
    int w8_ = wave; asm volatile("" : "+s"(w8_)); \
    k0 = w8_ < 4 ? 2 * w8_ : 4 + w8_; k1 = w8_ < 4 ? k0 + 1 : 10 + (w8_ >> 1); mw = w8_ < 4 ? 0 : w8_ & 1; \
    xl0 = sbase + w8_ * (4 * M_XT); \
    ws[0] = w_ + (WT_OFF + (((fw * 2 + half) * WH + k0 * WK + (fw & 1) * 8192 + 2048) ^ cbit)); ws[1] = ws[0] + (k1 - k0) * WK; \
    Yh = w_ + YH_OFF; cnt = (unsigned*)(w_ + CN_OFF); \
    ys = Yh + (Q * YQ + (((pm >> ((Q * 37) >> 8)) ^ (Q >> 1)) & 1) * 8192 + (fw >> 3) * 16384 + (fw & 7) * 1024 + half * 512); \
    flp = cnt + (G * 8 + wave) * CN_G + (fw * 2 + half);         \
    asm volatile("" : "+v"(vL)); \
    lane = vL >> 4; g = lane >> 4; r = lane & 15; q = (lane >> 2) & 3; p = lane & 3; \
  } while (0)

  unsigned zr[2], yv, to[4];
  u32x10 dv[2];
  f32x4 tacc[4][2][2];
#define SETUP_Z() do { \
    zr[0] = sbase + M_RB + wave * (4 * M_ZT) + (2 * g + (q >> 1)) * 64 + p * 16 + (q & 1) * 8; zr[1] = zr[0] + (g == 3 ? 384 : 512); \
    yv = (g & 1) * YT + (((r >> 1) ^ (fw & 3)) << 6) + (r & 1) * 32 + (g >> 1) * 16; \
  } while (0)
#define TDV(cc) do { _Pragma("unroll") for (int c_ = 0; c_ < 2; ++c_) { const u32x4 a_ = xr[cc][c_][0], b_ = xr[cc][c_][1]; \
      dv[c_] = u32x10{a_[0], a_[1], a_[2], a_[3], b_[0], b_[1], b_[2], b_[3], xh[cc][c_][0], xh[cc][c_][1]}; } \
    asm volatile("" : "+v"(dv[0]), "+v"(dv[1])); } while (0)
#define TMF(htl, chg, ct) do { const u32x4 b_ = __builtin_shufflevector(dv[chg], dv[chg], 2 * (htl), 2 * (htl) + 1, 2 * (htl) + 2, 2 * (htl) + 3); \
    MM(tacc[htl][ct][chg], fh[ct][0], b_, H_LO ? mfma32(fh[ct][1], b_, Z4) : Z4); } while (0)
#define TCV(i, htl, e) do { to[i] = pack2(tacc[htl][(e) >> 2][0][(e) & 3], tacc[htl][(e) >> 2][1][(e) & 3]); \
    if ((e) == 6) asm volatile("" :: "v"(to[i]), "v"(tacc[htl][1][0]), "v"(tacc[htl][1][1])); } while (0)
#define TM0(cc) asm volatile("s_mov_b32 m0, %0\n\ts_nop 0" :: "s"(xl0 + ((cc) == 1 ? 8192 : 0)) : "memory")
#define TWR(i, cc, htl, e) asm volatile("ds_write_addtid_b32 %0 offset:%1" :: "v"(to[i]), "i"(((cc) == 1 ? M_RB - 8192 : 0) + (htl) * M_XT + (e) * 256) : "memory")
#define TABD() DMA(sbase + M_TAB + wave * 1024, vL, tabp + 8192 + wave * 1024, 0)
#define TABI() do { const unsigned a_ = sbase + M_TAB + (fw == 16 ? 2048 : 0) + vL; DSR(fi[0], a_, 0); if (H_LO) DSR(fi[1], a_, 1024); } while (0)
#define TABG() do { const unsigned a_ = sbase + M_TAB + vL; DSR(gi[0][0], a_, 4096); DSR(gi[0][1], a_, 5120); DSR(gi[1][0], a_, 6144); DSR(gi[1][1], a_, 7168); } while (0)
  u32x2 zd[2][4];
#define ZRD(s, zo) do { TRR(zd[s][0], zr[0], zo); TRR(zd[s][1], zr[1], zo); TRR(zd[s][2], zr[0], (zo) + M_ZT); TRR(zd[s][3], zr[1], (zo) + M_ZT); } while (0)
  f32x4 zy[2][2];
  u32x4 zo;
#define ZIW(s, n) WAIT4(n, zd[s][0], zd[s][1], zd[s][2], zd[s][3])
#define ZIM(s, t) do { const u32x4 a_ = cat(zd[s][2 * (t)], zd[s][2 * (t) + 1]); MM(zy[s][t], a_, fi[0], H_LO ? mfma32(a_, fi[1], Z4) : Z4); } while (0)
  unsigned zc[4];
#define ZIC(s, i) do { if ((i) < 2) { zc[2 * (i)] = pack2(zy[s][0][2 * (i)], zy[s][0][2 * (i) + 1]); zc[2 * (i) + 1] = pack2(zy[s][1][2 * (i)], zy[s][1][2 * (i) + 1]); \
      PIN(zc[2 * (i)]); PIN(zc[2 * (i) + 1]); } \
    else { const auto s0_ = __builtin_amdgcn_permlane16_swap(zc[0], zc[1], false, false), s1_ = __builtin_amdgcn_permlane16_swap(zc[2], zc[3], false, false); \
      zo = u32x4{s0_[0], s1_[0], s0_[1], s1_[1]}; PIN(zo); } } while (0)
#define ZINV2(p_) do { ZIW(0, 4); ZIM(0, 0); ZIM(0, 1); ZIW(1, 0); ZIM(1, 0); ZIM(1, 1); SB0(); \
    ZIC(0, 0); ZIC(0, 1); ZIC(0, 2); YST(zo, ys + (p_) * YG); ZIC(1, 0); ZIC(1, 1); ZIC(1, 2); YST(zo, ys + (p_) * YG + 2 * YT); } while (0)
#define flc (cnt + (min(L, I_NWG - 1) >> 2) * CN_G)
#define src (Yh + (((L >> 2) * YQ + (L & 3) * YT + ((L >> 3) & 1) * 8192) ^ cbit))
  unsigned pv, st;
#define POLL(s_) asm volatile("s_cmp_lg_u32 %3, 7\n\ts_cbranch_scc1 2f\n\tglobal_load_dword %0, %1, %2 sc1\n2:" \
    : "=v"(pv) : "v"(min(vL >> 2, 132u)), "s"(flc + ((s_) - 1) * CN_P), "s"(wave) : "memory", "scc")
#define CHECK(n, s_) asm volatile("s_mov_b32 %[sp], 0\n\ts_waitcnt vmcnt(%[nn])\n\ts_cmp_lg_u32 %[wv], 7\n\ts_cbranch_scc1 2f\n" \
    "1:\n\tv_cmp_ne_u32 vcc, 1, %[v]\n\ts_nop 4\n\ts_cbranch_vccz 2f\n\t" \
    "s_add_u32 %[sp], %[sp], 1\n\ts_cmp_ge_u32 %[sp], %[lim]\n\ts_cbranch_scc1 2f\n\ts_sleep %[sl]\n\t" \
    "global_load_dword %[v], %[z], %[p] sc1\n\ts_waitcnt vmcnt(0)\n\ts_branch 1b\n2:" \
    : [v] "+v"(pv), [sp] "=&s"(st) : [z] "v"(min(vL >> 2, 132u)), [p] "s"(flc + ((s_) - 1) * CN_P), [lim] "i"(SPIN_MAX), [nn] "i"(n), [sl] "i"(POLL_SLEEP), [wv] "s"(wave) : "memory", "scc", "vcc")

#define srw (src + wave * 1024)
#define il (sbase + M_RA + wave * 1024)
#define IDMA(s_) do { asm volatile("s_cmp_lg_u32 %3, 0\n\ts_cbranch_scc1 1f\n\ts_mov_b32 m0, %0\n\ts_nop 0\n\tglobal_load_lds_dwordx4 %1, %2 sc1\n1:" \
      :: "s"(il + (s_) * YS + 16384), "v"(vL), "s"(srw + (s_) * YG + 32768), "s"(wave) : "memory", "scc"); \
    DMA1(il + (s_) * YS, vL, srw + (s_) * YG, 0); DMA1(il + (s_) * YS + 8192, vL, srw + (s_) * YG + 16384, 0); } while (0)
  {
    u32x4 wf[36], xt[3][2][2][2];
    f32x4 aP[3][2][2], aQ[3][2][2], aI[3][2][2];
    unsigned xa[2], xb[2], za[2];
#define SETUP_M() do { \
      xa[0] = sbase + r * M_XT + g * 16 + k0 * 128; xa[1] = xa[0] + (k1 - k0) * 128 + mw * (16 * M_XT); xb[0] = xa[0] + M_RB; xb[1] = xa[1] + M_RB; \
      za[0] = sbase + M_RB + r * M_ZT + g * 16 + k0 * 64; za[1] = za[0] + (k1 - k0) * 64 + mw * (16 * M_ZT); \
    } while (0)
#define XRD(cc, kk, ri, mt) DSR(xt[cc][kk][ri][mt], ((cc) == 1 ? xb : xa)[kk], (ri) * 64 + (mt) * (16 * M_XT))
    u32x4 zw;
    float zs[4];
#define ZC(p_, kk, mt, i) do { if ((i) < 2) { zs[2 * (i)] = aP[p_][kk][mt][2 * (i)] - aQ[p_][kk][mt][2 * (i)]; asm volatile("" : "+v"(zs[2 * (i)])); \
      zs[2 * (i) + 1] = aP[p_][kk][mt][2 * (i) + 1] - aQ[p_][kk][mt][2 * (i) + 1]; PIN(zs[2 * (i) + 1]); } \
    else if ((i) == 2) { zw[2] = pack2(aI[p_][kk][mt][0], aI[p_][kk][mt][1]); zw[3] = pack2(aI[p_][kk][mt][2], aI[p_][kk][mt][3]); PIN(zw[2]); PIN(zw[3]); } \
    else { zw[0] = pack2(zs[0], zs[1]); zw[1] = pack2(zs[2], zs[3]); } } while (0)
#define ZW(p_, kk, mt) DSW(za[kk] + ((p_) == 2 ? M_Z1 : 0), zw, ((p_) ? M_Z1 : 0) + (mt) * (16 * M_ZT))
    if (wave < 4) {
    XPIN();
    __builtin_amdgcn_s_setprio(3);
    XLD(0, 0, 0); SB0();
    XLD(0, 1, 0); SB0();
    TABF();
    XLD(0, 0, 1); SB0();
    XLD(0, 1, 1); SB0();
    XLD(0, 0, 2); SB0();
    XLD(0, 1, 2); SB0();
    __builtin_amdgcn_s_setprio(2);
    SETUP_X(); SB0();
    XLD(1, 0, 0); SB0();
    XLD(1, 1, 0); SB0();
    XLD(1, 0, 1); SB0();
    XLD(1, 1, 1); SB0();
    XLD(1, 0, 2); SB0();
    XLD(1, 1, 2); SB0();
    __builtin_amdgcn_s_setprio(0);
    SB0(); SETUP_P(); SETUP_M(); SETUP_Z(); SB0();
    VMW(6); SB0();
    asm volatile("" : XC_(fh[0][0]), XC_(fh[1][0])); SB0();
    STAMP(1);
    TDV(0); SB0();
    TMF(0, 0, 0); SB0();
    TMF(0, 1, 0); SB0();
    XLD(2, 0, 0); SB0();
    TMF(0, 0, 1); SB0();
    TMF(0, 1, 1); SB0();
    XLD(2, 1, 0); SB0();
    TMF(1, 0, 0); SB0();
    TCV(0, 0, 0); SB0();
    TCV(1, 0, 1); SB0();
    TM0(0);
    TWR(0, 0, 0, 0); SB0();
    TCV(2, 0, 2); SB0();
    TMF(1, 1, 0); SB0();
    XLD(2, 0, 1); SB0();
    TWR(1, 0, 0, 1); SB0();
    TCV(3, 0, 3); SB0();
    TWR(2, 0, 0, 2); SB0();
    TMF(1, 0, 1); SB0();
    TCV(0, 0, 4); SB0();
    TWR(3, 0, 0, 3); SB0();
    TCV(1, 0, 5); SB0();
    TWR(0, 0, 0, 4); SB0();
    TMF(1, 1, 1); SB0();
    XLD(2, 1, 1); SB0();
    TCV(2, 0, 6); SB0();
    TWR(1, 0, 0, 5); SB0();
    TWR(2, 0, 0, 6); SB0();
    TMF(2, 0, 0); SB0();
    TCV(3, 1, 0); SB0();
    TCV(0, 1, 1); SB0();
    TWR(3, 0, 1, 0); SB0();
    TCV(1, 1, 2); SB0();
    TMF(2, 1, 0); SB0();
    XLD(2, 0, 2); SB0();
    TWR(0, 0, 1, 1); SB0();
    TCV(2, 1, 3); SB0();
    TWR(1, 0, 1, 2); SB0();
    TMF(2, 0, 1); SB0();
    TCV(3, 1, 4); SB0();
    TWR(2, 0, 1, 3); SB0();
    TCV(0, 1, 5); SB0();
    TWR(3, 0, 1, 4); SB0();
    TMF(2, 1, 1); SB0();
    XLD(2, 1, 2); SB0();
    TCV(1, 1, 6); SB0();
    TWR(0, 0, 1, 5); SB0();
    TWR(1, 0, 1, 6); SB0();
    TMF(3, 0, 0); SB0();
    TCV(2, 2, 0); SB0();
    TCV(3, 2, 1); SB0();
    TWR(2, 0, 2, 0); SB0();
    TCV(0, 2, 2); SB0();
    TMF(3, 1, 0); SB0();
    TWR(3, 0, 2, 1); SB0();
    TCV(1, 2, 3); SB0();
    TWR(0, 0, 2, 2); SB0();
    TMF(3, 0, 1); SB0();
    WLD(wf[0], vL, ws[0] + 0 * WP, -2048); SB0();
    TCV(2, 2, 4); SB0();
    TWR(1, 0, 2, 3); SB0();
    TCV(3, 2, 5); SB0();
    TMF(3, 1, 1); SB0();
    TWR(2, 0, 2, 4); SB0();
    TCV(0, 2, 6); SB0();
    TWR(3, 0, 2, 5); SB0();
    TWR(0, 0, 2, 6); SB0();
    VMW(7); SB0();
    TDV(1); SB0();
    TMF(0, 0, 0); SB0();
    TCV(1, 3, 0); SB0();
    TCV(2, 3, 1); SB0();
    TWR(1, 0, 3, 0); SB0();
    TCV(3, 3, 2); SB0();
    TMF(0, 1, 0); SB0();
    WLD(wf[1], vL, ws[0] + 0 * WP, -1024); SB0();
    TWR(2, 0, 3, 1); SB0();
    TCV(0, 3, 3); SB0();
    TWR(3, 0, 3, 2); SB0();
    TMF(0, 0, 1); SB0();
    TCV(1, 3, 4); SB0();
    TWR(0, 0, 3, 3); SB0();
    TCV(2, 3, 5); SB0();
    TWR(1, 0, 3, 4); SB0();
    TMF(0, 1, 1); SB0();
    TCV(3, 3, 6); SB0();
    TWR(2, 0, 3, 5); SB0();
    TWR(3, 0, 3, 6); SB0();
    TMF(1, 0, 0); SB0();
    TABD(); SB0();
    TCV(0, 0, 0); SB0();
    TCV(1, 0, 1); SB0();
    TM0(1);
    TWR(0, 1, 0, 0); SB0();
    TMF(1, 1, 0); SB0();
    TCV(2, 0, 2); SB0();
    TWR(1, 1, 0, 1); SB0();
    TCV(3, 0, 3); SB0();
    TWR(2, 1, 0, 2); SB0();
    TMF(1, 0, 1); SB0();
    TCV(0, 0, 4); SB0();
    TWR(3, 1, 0, 3); SB0();
    TCV(1, 0, 5); SB0();
    TWR(0, 1, 0, 4); SB0();
    TMF(1, 1, 1); SB0();
    WLD(wf[2], vL, ws[1] + 0 * WP, -2048); SB0();
    TCV(2, 0, 6); SB0();
    TWR(1, 1, 0, 5); SB0();
    TWR(2, 1, 0, 6); SB0();
    asm volatile("s_waitcnt lgkmcnt(7)" ::: "memory");
    BAR(); SB0();
    STAMP(2);
    XRD(0, 0, 0, 0);
    XRD(0, 0, 1, 0);
    XRD(0, 0, 0, 1);
    XRD(0, 0, 1, 1);
    XRD(0, 1, 0, 0);
    XRD(0, 1, 1, 0);
    XRD(0, 1, 0, 1);
    XRD(0, 1, 1, 1);
    SB0();
    TMF(2, 0, 0); SB0();
    TCV(3, 1, 0); SB0();
    TCV(0, 1, 1); SB0();
    TWR(3, 1, 1, 0); SB0();
    TCV(1, 1, 2); SB0();
    TMF(2, 1, 0); SB0();
    TWR(0, 1, 1, 1); SB0();
    TCV(2, 1, 3); SB0();
    TWR(1, 1, 1, 2); SB0();
    TMF(2, 0, 1); SB0();
    WLD(wf[3], vL, ws[1] + 0 * WP, -1024); SB0();
    TCV(3, 1, 4); SB0();
    TWR(2, 1, 1, 3); SB0();
    TCV(0, 1, 5); SB0();
    TMF(2, 1, 1); SB0();
    TWR(3, 1, 1, 4); SB0();
    TCV(1, 1, 6); SB0();
    TWR(0, 1, 1, 5); SB0();
    TWR(1, 1, 1, 6); SB0();
    TMF(3, 0, 0); SB0();
    TCV(2, 2, 0); SB0();
    TCV(3, 2, 1); SB0();
    TWR(2, 1, 2, 0); SB0();
    TMF(3, 1, 0); SB0();
    WLD(wf[4], vL, ws[0] + 0 * WP, 0); SB0();
    TCV(0, 2, 2); SB0();
    TWR(3, 1, 2, 1); SB0();
    TMF(3, 0, 1); SB0();
    TCV(1, 2, 3); SB0();
    TWR(0, 1, 2, 2); SB0();
    TCV(2, 2, 4); SB0();
    TMF(3, 1, 1); SB0();
    TWR(1, 1, 2, 3); SB0();
    TCV(3, 2, 5); SB0();
    TWR(2, 1, 2, 4); SB0();
    WAITV(4, wf[0], wf[1]); SB0();
    asm volatile("s_waitcnt lgkmcnt(15)" : XC_(xt[0][0][0][0]), XC_(xt[0][0][1][0]) :: "memory"); SB0();
    MM(aP[0][0][0], wf[0], xt[0][0][0][0], Z4); SB0();
    WLD(wf[5], vL, ws[0] + 0 * WP, 1024); SB0();
    TCV(0, 2, 6); SB0();
    TWR(3, 1, 2, 5); SB0();
    MM(aQ[0][0][0], wf[1], xt[0][0][1][0], Z4); SB0();
    TWR(0, 1, 2, 6); SB0();
    TCV(1, 3, 0); SB0();
    TCV(2, 3, 1); SB0();
    MM(aI[0][0][0], wf[0], xt[0][0][1][0], Z4); SB0();
    TWR(1, 1, 3, 0); SB0();
    TCV(3, 3, 2); SB0();
    TWR(2, 1, 3, 1); SB0();
    MM(aI[0][0][0], wf[1], xt[0][0][0][0], aI[0][0][0]); SB0();
    WLD(wf[6], vL, ws[1] + 0 * WP, 0); SB0();
    TCV(0, 3, 3); SB0();
    TWR(3, 1, 3, 2); SB0();
    asm volatile("" : XC_(xt[0][0][0][1]), XC_(xt[0][0][1][1])); SB0();
    MM(aP[0][0][1], wf[0], xt[0][0][0][1], Z4); SB0();
    TCV(1, 3, 4); SB0();
    TWR(0, 1, 3, 3); SB0();
    TCV(2, 3, 5); SB0();
    MM(aQ[0][0][1], wf[1], xt[0][0][1][1], Z4); SB0();
    TWR(1, 1, 3, 4); SB0();
    TCV(3, 3, 6); SB0();
    TWR(2, 1, 3, 5); SB0();
    MM(aI[0][0][1], wf[0], xt[0][0][1][1], Z4); SB0();
    WLD(wf[7], vL, ws[1] + 0 * WP, 1024); SB0();
    TWR(3, 1, 3, 6); SB0();
    MM(aI[0][0][1], wf[1], xt[0][0][0][1], aI[0][0][1]); SB0();
    asm volatile("s_waitcnt lgkmcnt(0)" ::: "memory");
    BAR(); SB0();
    STAMP(3);
    XRD(1, 0, 0, 0);
    XRD(1, 0, 1, 0);
    XRD(1, 0, 0, 1);
    XRD(1, 0, 1, 1);
    XRD(1, 1, 0, 0);
    XRD(1, 1, 1, 0);
    XRD(1, 1, 0, 1);
    XRD(1, 1, 1, 1);
    SB0();
    VMW(9); SB0();
    TDV(2); SB0();
    TMF(0, 0, 0); SB0();
    TMF(0, 1, 0); SB0();
    WLD(wf[8], vL, ws[0] + 1 * WP, -2048); SB0();
    TMF(0, 0, 1); SB0();
    TMF(0, 1, 1); SB0();
    WAITV(5, wf[2], wf[3]); SB0();
    asm volatile("" : XC_(xt[0][1][0][0]), XC_(xt[0][1][1][0])); SB0();
    MM(aP[0][1][0], wf[2], xt[0][1][0][0], Z4); SB0();
    WLD(wf[9], vL, ws[0] + 1 * WP, -1024); SB0();
    TCV(0, 0, 0); SB0();
    TCV(1, 0, 1); SB0();
    MM(aQ[0][1][0], wf[3], xt[0][1][1][0], Z4); SB0();
    TM0(2);
    TWR(0, 2, 0, 0); SB0();
    TCV(2, 0, 2); SB0();
    TWR(1, 2, 0, 1); SB0();
    MM(aI[0][1][0], wf[2], xt[0][1][1][0], Z4); SB0();
    TCV(3, 0, 3); SB0();
    TWR(2, 2, 0, 2); SB0();
    TCV(0, 0, 4); SB0();
    MM(aI[0][1][0], wf[3], xt[0][1][0][0], aI[0][1][0]); SB0();
    WLD(wf[10], vL, ws[0] + 0 * WP, 2048); SB0();
    TWR(3, 2, 0, 3); SB0();
    TCV(1, 0, 5); SB0();
    TMF(1, 0, 0); SB0();
    TWR(0, 2, 0, 4); SB0();
    TCV(2, 0, 6); SB0();
    TWR(1, 2, 0, 5); SB0();
    TMF(1, 1, 0); SB0();
    TWR(2, 2, 0, 6); SB0();
    TMF(1, 0, 1); SB0();
    WLD(wf[11], vL, ws[0] + 0 * WP, 3072); SB0();
    TMF(1, 1, 1); SB0();
    asm volatile("" : XC_(xt[0][1][0][1]), XC_(xt[0][1][1][1])); SB0();
    MM(aP[0][1][1], wf[2], xt[0][1][0][1], Z4); SB0();
    TCV(3, 1, 0); SB0();
    TCV(0, 1, 1); SB0();
    TWR(3, 2, 1, 0); SB0();
    MM(aQ[0][1][1], wf[3], xt[0][1][1][1], Z4); SB0();
    WLD(wf[12], vL, ws[1] + 0 * WP, 2048); SB0();
    TCV(1, 1, 2); SB0();
    TWR(0, 2, 1, 1); SB0();
    MM(aI[0][1][1], wf[2], xt[0][1][1][1], Z4); SB0();
    TCV(2, 1, 3); SB0();
    TWR(1, 2, 1, 2); SB0();
    TCV(3, 1, 4); SB0();
    MM(aI[0][1][1], wf[3], xt[0][1][0][1], aI[0][1][1]); SB0();
    TWR(2, 2, 1, 3); SB0();
    TCV(0, 1, 5); SB0();
    TWR(3, 2, 1, 4); SB0();
    TMF(2, 0, 0); SB0();
    WLD(wf[13], vL, ws[1] + 0 * WP, 3072); SB0();
    TCV(1, 1, 6); SB0();
    TWR(0, 2, 1, 5); SB0();
    TMF(2, 1, 0); SB0();
    TWR(1, 2, 1, 6); SB0();
    TMF(2, 0, 1); SB0();
    TMF(2, 1, 1); SB0();
    WLD(wf[14], vL, ws[0] + 1 * WP, 0); SB0();
    WAITV(9, wf[4], wf[5]); SB0();
    asm volatile("s_waitcnt lgkmcnt(15)" : XC_(xt[1][0][0][0]), XC_(xt[1][0][1][0]) :: "memory"); SB0();
    MM(aP[0][0][0], wf[4], xt[1][0][0][0], aP[0][0][0]); SB0();
    TCV(2, 2, 0); SB0();
    TCV(3, 2, 1); SB0();
    TWR(2, 2, 2, 0); SB0();
    MM(aQ[0][0][0], wf[5], xt[1][0][1][0], aQ[0][0][0]); SB0();
    TCV(0, 2, 2); SB0();
    TWR(3, 2, 2, 1); SB0();
    TCV(1, 2, 3); SB0();
    MM(aI[0][0][0], wf[4], xt[1][0][1][0], aI[0][0][0]); SB0();
    WLD(wf[15], vL, ws[0] + 1 * WP, 1024); SB0();
    TWR(0, 2, 2, 2); SB0();
    TCV(2, 2, 4); SB0();
    MM(aI[0][0][0], wf[5], xt[1][0][0][0], aI[0][0][0]); SB0();
    TWR(1, 2, 2, 3); SB0();
    TCV(3, 2, 5); SB0();
    TWR(2, 2, 2, 4); SB0();
    TMF(3, 0, 0); SB0();
    TCV(0, 2, 6); SB0();
    TWR(3, 2, 2, 5); SB0();
    TWR(0, 2, 2, 6); SB0();
    TMF(3, 1, 0); SB0();
    WLD(wf[16], vL, ws[0] + 1 * WP, 2048); SB0();
    TMF(3, 0, 1); SB0();
    TMF(3, 1, 1); SB0();
    asm volatile("" : XC_(xt[1][0][0][1]), XC_(xt[1][0][1][1])); SB0();
    MM(aP[0][0][1], wf[4], xt[1][0][0][1], aP[0][0][1]); SB0();
    WLD(wf[17], vL, ws[0] + 1 * WP, 3072); SB0();
    TCV(1, 3, 0); SB0();
    TCV(2, 3, 1); SB0();
    MM(aQ[0][0][1], wf[5], xt[1][0][1][1], aQ[0][0][1]); SB0();
    TWR(1, 2, 3, 0); SB0();
    TCV(3, 3, 2); SB0();
    TWR(2, 2, 3, 1); SB0();
    MM(aI[0][0][1], wf[4], xt[1][0][1][1], aI[0][0][1]); SB0();
    TCV(0, 3, 3); SB0();
    TWR(3, 2, 3, 2); SB0();
    TCV(1, 3, 4); SB0();
    MM(aI[0][0][1], wf[5], xt[1][0][0][1], aI[0][0][1]); SB0();
    WLD(wf[18], vL, ws[1] + 1 * WP, -2048); SB0();
    TWR(0, 2, 3, 3); SB0();
    TCV(2, 3, 5); SB0();
    WAITV(11, wf[6], wf[7]); SB0();
    asm volatile("" : XC_(xt[1][1][0][0]), XC_(xt[1][1][1][0])); SB0();
    MM(aP[0][1][0], wf[6], xt[1][1][0][0], aP[0][1][0]); SB0();
    TWR(1, 2, 3, 4); SB0();
    TCV(3, 3, 6); SB0();
    TWR(2, 2, 3, 5); SB0();
    MM(aQ[0][1][0], wf[7], xt[1][1][1][0], aQ[0][1][0]); SB0();
    TWR(3, 2, 3, 6); SB0();
    MM(aI[0][1][0], wf[6], xt[1][1][1][0], aI[0][1][0]); SB0();
    WLD(wf[19], vL, ws[1] + 1 * WP, -1024); SB0();
    MM(aI[0][1][0], wf[7], xt[1][1][0][0], aI[0][1][0]); SB0();
    asm volatile("s_waitcnt lgkmcnt(15)" : XC_(xt[1][1][0][1]), XC_(xt[1][1][1][1]) :: "memory"); SB0();
    MM(aP[0][1][1], wf[6], xt[1][1][0][1], aP[0][1][1]); SB0();
    MM(aQ[0][1][1], wf[7], xt[1][1][1][1], aQ[0][1][1]); SB0();
    WLD(wf[20], vL, ws[1] + 1 * WP, 0); SB0();
    MM(aI[0][1][1], wf[6], xt[1][1][1][1], aI[0][1][1]); SB0();
    MM(aI[0][1][1], wf[7], xt[1][1][0][1], aI[0][1][1]); SB0();
    asm volatile("s_waitcnt lgkmcnt(0)" ::: "memory");
    BAR(); SB0();
    STAMP(4);
    XRD(2, 0, 0, 0);
    XRD(2, 0, 1, 0);
    XRD(2, 0, 0, 1);
    XRD(2, 0, 1, 1);
    XRD(2, 1, 0, 0);
    XRD(2, 1, 1, 0);
    XRD(2, 1, 0, 1);
    XRD(2, 1, 1, 1);
    SB0();
    WAITV(11, wf[8], wf[9]); SB0();
    MM(aP[1][0][0], wf[8], xt[0][0][0][0], Z4); SB0();
    WLD(wf[21], vL, ws[1] + 1 * WP, 1024); SB0();
    MM(aQ[1][0][0], wf[9], xt[0][0][1][0], Z4); SB0();
    MM(aI[1][0][0], wf[8], xt[0][0][1][0], Z4); SB0();
    MM(aI[1][0][0], wf[9], xt[0][0][0][0], aI[1][0][0]); SB0();
    WLD(wf[22], vL, ws[1] + 1 * WP, 2048); SB0();
    MM(aP[1][0][1], wf[8], xt[0][0][0][1], Z4); SB0();
    MM(aQ[1][0][1], wf[9], xt[0][0][1][1], Z4); SB0();
    MM(aI[1][0][1], wf[8], xt[0][0][1][1], Z4); SB0();
    WLD(wf[23], vL, ws[1] + 1 * WP, 3072); SB0();
    MM(aI[1][0][1], wf[9], xt[0][0][0][1], aI[1][0][1]); SB0();
    WAITV(12, wf[10], wf[11]); SB0();
    asm volatile("s_waitcnt lgkmcnt(6)" : XC_(xt[2][0][0][0]), XC_(xt[2][0][1][0]) :: "memory"); SB0();
    MM(aP[0][0][0], wf[10], xt[2][0][0][0], aP[0][0][0]); SB0();
    MM(aQ[0][0][0], wf[11], xt[2][0][1][0], aQ[0][0][0]); SB0();
    WLD(wf[24], vL, ws[0] + 2 * WP, -2048); SB0();
    MM(aI[0][0][0], wf[10], xt[2][0][1][0], aI[0][0][0]); SB0();
    MM(aI[0][0][0], wf[11], xt[2][0][0][0], aI[0][0][0]); SB0();
    asm volatile("s_waitcnt lgkmcnt(4)" : XC_(xt[2][0][0][1]), XC_(xt[2][0][1][1]) :: "memory"); SB0();
    MM(aP[0][0][1], wf[10], xt[2][0][0][1], aP[0][0][1]); SB0();
    WLD(wf[25], vL, ws[0] + 2 * WP, -1024); SB0();
    MM(aQ[0][0][1], wf[11], xt[2][0][1][1], aQ[0][0][1]); SB0();
    ZC(0, 0, 0, 0); SB0();
    MM(aI[0][0][1], wf[10], xt[2][0][1][1], aI[0][0][1]); SB0();
    ZC(0, 0, 0, 1); SB0();
    MM(aI[0][0][1], wf[11], xt[2][0][0][1], aI[0][0][1]); SB0();
    ZC(0, 0, 0, 2); SB0();
    WAITV(12, wf[12], wf[13]); SB0();
    asm volatile("s_waitcnt lgkmcnt(2)" : XC_(xt[2][1][0][0]), XC_(xt[2][1][1][0]) :: "memory"); SB0();
    MM(aP[0][1][0], wf[12], xt[2][1][0][0], aP[0][1][0]); SB0();
    WLD(wf[26], vL, ws[0] + 2 * WP, 0); SB0();
    ZC(0, 0, 0, 3); SB0();
    MM(aQ[0][1][0], wf[13], xt[2][1][1][0], aQ[0][1][0]); SB0();
    ZW(0, 0, 0); SB0();
    ZC(0, 0, 1, 0); SB0();
    MM(aI[0][1][0], wf[12], xt[2][1][1][0], aI[0][1][0]); SB0();
    ZC(0, 0, 1, 1); SB0();
    MM(aI[0][1][0], wf[13], xt[2][1][0][0], aI[0][1][0]); SB0();
    WLD(wf[27], vL, ws[0] + 2 * WP, 1024); SB0();
    ZC(0, 0, 1, 2); SB0();
    asm volatile("s_waitcnt lgkmcnt(1)" : XC_(xt[2][1][0][1]), XC_(xt[2][1][1][1]) :: "memory"); SB0();
    MM(aP[0][1][1], wf[12], xt[2][1][0][1], aP[0][1][1]); SB0();
    ZC(0, 0, 1, 3); SB0();
    ZW(0, 0, 1); SB0();
    MM(aQ[0][1][1], wf[13], xt[2][1][1][1], aQ[0][1][1]); SB0();
    ZC(0, 1, 0, 0); SB0();
    MM(aI[0][1][1], wf[12], xt[2][1][1][1], aI[0][1][1]); SB0();
    ZC(0, 1, 0, 1); SB0();
    MM(aI[0][1][1], wf[13], xt[2][1][0][1], aI[0][1][1]); SB0();
    ZC(0, 1, 0, 2); SB0();
    WAITV(12, wf[14], wf[15]); SB0();
    MM(aP[1][0][0], wf[14], xt[1][0][0][0], aP[1][0][0]); SB0();
    WLD(wf[28], vL, ws[0] + 2 * WP, 2048); SB0();
    ZC(0, 1, 0, 3); SB0();
    MM(aQ[1][0][0], wf[15], xt[1][0][1][0], aQ[1][0][0]); SB0();
    ZW(0, 1, 0); SB0();
    ZC(0, 1, 1, 0); SB0();
    MM(aI[1][0][0], wf[14], xt[1][0][1][0], aI[1][0][0]); SB0();
    ZC(0, 1, 1, 1); SB0();
    MM(aI[1][0][0], wf[15], xt[1][0][0][0], aI[1][0][0]); SB0();
    WLD(wf[29], vL, ws[0] + 2 * WP, 3072); SB0();
    ZC(0, 1, 1, 2); SB0();
    MM(aP[1][0][1], wf[14], xt[1][0][0][1], aP[1][0][1]); SB0();
    ZC(0, 1, 1, 3); SB0();
    ZW(0, 1, 1); SB0();
    MM(aQ[1][0][1], wf[15], xt[1][0][1][1], aQ[1][0][1]); SB0();
    MM(aI[1][0][1], wf[14], xt[1][0][1][1], aI[1][0][1]); SB0();
    MM(aI[1][0][1], wf[15], xt[1][0][0][1], aI[1][0][1]); SB0();
    STAMP(12);
    LGW0(); BAR(); SB0();
    STAMP(13);
    TABI();
    ZRD(0, 0); ZRD(1, 2 * M_ZT); SB0();
    WAITV(12, wf[16], wf[17]); SB0();
    MM(aP[1][0][0], wf[16], xt[2][0][0][0], aP[1][0][0]); SB0();
    WLD(wf[30], vL, ws[1] + 2 * WP, -2048); SB0();
    MM(aQ[1][0][0], wf[17], xt[2][0][1][0], aQ[1][0][0]); SB0();
    MM(aI[1][0][0], wf[16], xt[2][0][1][0], aI[1][0][0]); SB0();
    MM(aI[1][0][0], wf[17], xt[2][0][0][0], aI[1][0][0]); SB0();
    WLD(wf[31], vL, ws[1] + 2 * WP, -1024); SB0();
    ZIW(0, 4); SB0();
    asm volatile("" : XC_(fi[0])); SB0();
    ZIM(0, 0); SB0();
    ZIM(0, 1); SB0();
    ZC(1, 0, 0, 0); SB0();
    ZIW(1, 0); SB0();
    ZIM(1, 0); SB0();
    ZC(1, 0, 0, 1); SB0();
    ZIM(1, 1); SB0();
    ZC(1, 0, 0, 2); SB0();
    MM(aP[1][0][1], wf[16], xt[2][0][0][1], aP[1][0][1]); SB0();
    MM(aQ[1][0][1], wf[17], xt[2][0][1][1], aQ[1][0][1]); SB0();
    ZIC(0, 0); SB0();
    MM(aI[1][0][1], wf[16], xt[2][0][1][1], aI[1][0][1]); SB0();
    ZIC(0, 1); SB0();
    MM(aI[1][0][1], wf[17], xt[2][0][0][1], aI[1][0][1]); SB0();
    ZIC(0, 2); SB0();
    WAITV(12, wf[18], wf[19]); SB0();
    MM(aP[1][1][0], wf[18], xt[0][1][0][0], Z4); SB0();
    WLD(wf[32], vL, ws[1] + 2 * WP, 0); SB0();
    YST(zo, ys + 0 * YT + 0 * YG); SB0();
    MM(aQ[1][1][0], wf[19], xt[0][1][1][0], Z4); SB0();
    ZIC(1, 0); SB0();
    MM(aI[1][1][0], wf[18], xt[0][1][1][0], Z4); SB0();
    ZIC(1, 1); SB0();
    MM(aI[1][1][0], wf[19], xt[0][1][0][0], aI[1][1][0]); SB0();
    WLD(wf[33], vL, ws[1] + 2 * WP, 1024); SB0();
    ZIC(1, 2); SB0();
    MM(aP[1][1][1], wf[18], xt[0][1][0][1], Z4); SB0();
    YST(zo, ys + 2 * YT + 0 * YG); SB0();
    ZC(1, 0, 0, 3); SB0();
    MM(aQ[1][1][1], wf[19], xt[0][1][1][1], Z4); SB0();
    ZW(1, 0, 0); SB0();
    ZC(1, 0, 1, 0); SB0();
    MM(aI[1][1][1], wf[18], xt[0][1][1][1], Z4); SB0();
    ZC(1, 0, 1, 1); SB0();
    MM(aI[1][1][1], wf[19], xt[0][1][0][1], aI[1][1][1]); SB0();
    ZC(1, 0, 1, 2); SB0();
    WAITV(14, wf[20], wf[21]); SB0();
    MM(aP[1][1][0], wf[20], xt[1][1][0][0], aP[1][1][0]); SB0();
    WLD(wf[34], vL, ws[1] + 2 * WP, 2048); SB0();
    ZC(1, 0, 1, 3); SB0();
    MM(aQ[1][1][0], wf[21], xt[1][1][1][0], aQ[1][1][0]); SB0();
    ZW(1, 0, 1); SB0();
    MM(aI[1][1][0], wf[20], xt[1][1][1][0], aI[1][1][0]); SB0();
    MM(aI[1][1][0], wf[21], xt[1][1][0][0], aI[1][1][0]); SB0();
    WLD(wf[35], vL, ws[1] + 2 * WP, 3072); SB0();
    MM(aP[1][1][1], wf[20], xt[1][1][0][1], aP[1][1][1]); SB0();
    MM(aQ[1][1][1], wf[21], xt[1][1][1][1], aQ[1][1][1]); SB0();
    MM(aI[1][1][1], wf[20], xt[1][1][1][1], aI[1][1][1]); SB0();
    MM(aI[1][1][1], wf[21], xt[1][1][0][1], aI[1][1][1]); SB0();
    WAITV(14, wf[22], wf[23]); SB0();
    MM(aP[1][1][0], wf[22], xt[2][1][0][0], aP[1][1][0]); SB0();
    MM(aQ[1][1][0], wf[23], xt[2][1][1][0], aQ[1][1][0]); SB0();
    MM(aI[1][1][0], wf[22], xt[2][1][1][0], aI[1][1][0]); SB0();
    MM(aI[1][1][0], wf[23], xt[2][1][0][0], aI[1][1][0]); SB0();
    MM(aP[1][1][1], wf[22], xt[2][1][0][1], aP[1][1][1]); SB0();
    MM(aQ[1][1][1], wf[23], xt[2][1][1][1], aQ[1][1][1]); SB0();
    ZC(1, 1, 0, 0); SB0();
    MM(aI[1][1][1], wf[22], xt[2][1][1][1], aI[1][1][1]); SB0();
    ZC(1, 1, 0, 1); SB0();
    MM(aI[1][1][1], wf[23], xt[2][1][0][1], aI[1][1][1]); SB0();
    ZC(1, 1, 0, 2); SB0();
    WAITV(12, wf[24], wf[25]); SB0();
    MM(aP[2][0][0], wf[24], xt[0][0][0][0], Z4); SB0();
    ZC(1, 1, 0, 3); SB0();
    ZW(1, 1, 0); SB0();
    MM(aQ[2][0][0], wf[25], xt[0][0][1][0], Z4); SB0();
    ZC(1, 1, 1, 0); SB0();
    MM(aI[2][0][0], wf[24], xt[0][0][1][0], Z4); SB0();
    ZC(1, 1, 1, 1); SB0();
    MM(aI[2][0][0], wf[25], xt[0][0][0][0], aI[2][0][0]); SB0();
    ZC(1, 1, 1, 2); SB0();
    MM(aP[2][0][1], wf[24], xt[0][0][0][1], Z4); SB0();
    ZC(1, 1, 1, 3); SB0();
    ZW(1, 1, 1); SB0();
    MM(aQ[2][0][1], wf[25], xt[0][0][1][1], Z4); SB0();
    MM(aI[2][0][1], wf[24], xt[0][0][1][1], Z4); SB0();
    MM(aI[2][0][1], wf[25], xt[0][0][0][1], aI[2][0][1]); SB0();
    STAMP(14);
    LGW0(); BAR(); SB0();
    STAMP(15);
    ZRD(0, M_Z1 + 0); ZRD(1, M_Z1 + 2 * M_ZT); SB0();
    WAITV(10, wf[26], wf[27]); SB0();
    MM(aP[2][0][0], wf[26], xt[1][0][0][0], aP[2][0][0]); SB0();
    MM(aQ[2][0][0], wf[27], xt[1][0][1][0], aQ[2][0][0]); SB0();
    MM(aI[2][0][0], wf[26], xt[1][0][1][0], aI[2][0][0]); SB0();
    MM(aI[2][0][0], wf[27], xt[1][0][0][0], aI[2][0][0]); SB0();
    ZIW(0, 4); SB0();
    ZIM(0, 0); SB0();
    ZIM(0, 1); SB0();
    ZIW(1, 0); SB0();
    ZIM(1, 0); SB0();
    ZIM(1, 1); SB0();
    MM(aP[2][0][1], wf[26], xt[1][0][0][1], aP[2][0][1]); SB0();
    MM(aQ[2][0][1], wf[27], xt[1][0][1][1], aQ[2][0][1]); SB0();
    ZIC(0, 0); SB0();
    MM(aI[2][0][1], wf[26], xt[1][0][1][1], aI[2][0][1]); SB0();
    ZIC(0, 1); SB0();
    MM(aI[2][0][1], wf[27], xt[1][0][0][1], aI[2][0][1]); SB0();
    ZIC(0, 2); SB0();
    WAITV(8, wf[28], wf[29]); SB0();
    MM(aP[2][0][0], wf[28], xt[2][0][0][0], aP[2][0][0]); SB0();
    YST(zo, ys + 0 * YT + 1 * YG); SB0();
    ZIC(1, 0); SB0();
    MM(aQ[2][0][0], wf[29], xt[2][0][1][0], aQ[2][0][0]); SB0();
    ZIC(1, 1); SB0();
    MM(aI[2][0][0], wf[28], xt[2][0][1][0], aI[2][0][0]); SB0();
    ZIC(1, 2); SB0();
    MM(aI[2][0][0], wf[29], xt[2][0][0][0], aI[2][0][0]); SB0();
    YST(zo, ys + 2 * YT + 1 * YG); SB0();
    MM(aP[2][0][1], wf[28], xt[2][0][0][1], aP[2][0][1]); SB0();
    MM(aQ[2][0][1], wf[29], xt[2][0][1][1], aQ[2][0][1]); SB0();
    ZC(2, 0, 0, 0); SB0();
    MM(aI[2][0][1], wf[28], xt[2][0][1][1], aI[2][0][1]); SB0();
    ZC(2, 0, 0, 1); SB0();
    MM(aI[2][0][1], wf[29], xt[2][0][0][1], aI[2][0][1]); SB0();
    ZC(2, 0, 0, 2); SB0();
    WAITV(8, wf[30], wf[31]); SB0();
    MM(aP[2][1][0], wf[30], xt[0][1][0][0], Z4); SB0();
    ZC(2, 0, 0, 3); SB0();
    ZW(2, 0, 0); SB0();
    MM(aQ[2][1][0], wf[31], xt[0][1][1][0], Z4); SB0();
    ZC(2, 0, 1, 0); SB0();
    MM(aI[2][1][0], wf[30], xt[0][1][1][0], Z4); SB0();
    ZC(2, 0, 1, 1); SB0();
    MM(aI[2][1][0], wf[31], xt[0][1][0][0], aI[2][1][0]); SB0();
    ZC(2, 0, 1, 2); SB0();
    MM(aP[2][1][1], wf[30], xt[0][1][0][1], Z4); SB0();
    ZC(2, 0, 1, 3); SB0();
    ZW(2, 0, 1); SB0();
    MM(aQ[2][1][1], wf[31], xt[0][1][1][1], Z4); SB0();
    MM(aI[2][1][1], wf[30], xt[0][1][1][1], Z4); SB0();
    MM(aI[2][1][1], wf[31], xt[0][1][0][1], aI[2][1][1]); SB0();
    WAITV(5, wf[32], wf[33]); SB0();
    MM(aP[2][1][0], wf[32], xt[1][1][0][0], aP[2][1][0]); SB0();
    MM(aQ[2][1][0], wf[33], xt[1][1][1][0], aQ[2][1][0]); SB0();
    MM(aI[2][1][0], wf[32], xt[1][1][1][0], aI[2][1][0]); SB0();
    MM(aI[2][1][0], wf[33], xt[1][1][0][0], aI[2][1][0]); SB0();
    MM(aP[2][1][1], wf[32], xt[1][1][0][1], aP[2][1][1]); SB0();
    MM(aQ[2][1][1], wf[33], xt[1][1][1][1], aQ[2][1][1]); SB0();
    MM(aI[2][1][1], wf[32], xt[1][1][1][1], aI[2][1][1]); SB0();
    MM(aI[2][1][1], wf[33], xt[1][1][0][1], aI[2][1][1]); SB0();
    WAITV(2, wf[34], wf[35]); SB0();
    MM(aP[2][1][0], wf[34], xt[2][1][0][0], aP[2][1][0]); SB0();
    MM(aQ[2][1][0], wf[35], xt[2][1][1][0], aQ[2][1][0]); SB0();
    MM(aI[2][1][0], wf[34], xt[2][1][1][0], aI[2][1][0]); SB0();
    MM(aI[2][1][0], wf[35], xt[2][1][0][0], aI[2][1][0]); SB0();
    MM(aP[2][1][1], wf[34], xt[2][1][0][1], aP[2][1][1]); SB0();
    MM(aQ[2][1][1], wf[35], xt[2][1][1][1], aQ[2][1][1]); SB0();
    ZC(2, 1, 0, 0); SB0();
    MM(aI[2][1][1], wf[34], xt[2][1][1][1], aI[2][1][1]); SB0();
    ZC(2, 1, 0, 1); SB0();
    MM(aI[2][1][1], wf[35], xt[2][1][0][1], aI[2][1][1]); SB0();
    ZC(2, 1, 0, 2); SB0();
    STAMP(5);
    ZC(2, 1, 0, 3); SB0();
    ZW(2, 1, 0); SB0();
    ZC(2, 1, 1, 0); SB0();
    ZC(2, 1, 1, 1); SB0();
    ZC(2, 1, 1, 2); SB0();
    ZC(2, 1, 1, 3); SB0();
    ZW(2, 1, 1); SB0();
    VMW(0); ARRIVE(flp + 0 * CN_P); SB0();
    LGW0(); BAR(); SB0();
    } else {
    XPIN();
    __builtin_amdgcn_s_setprio(3);
    XLD(0, 0, 0); SB0();
    XLD(0, 1, 0); SB0();
    TABF();
    XLD(0, 0, 1); SB0();
    XLD(0, 1, 1); SB0();
    XLD(0, 0, 2); SB0();
    XLD(0, 1, 2); SB0();
    __builtin_amdgcn_s_setprio(2);
    SETUP_X(); SB0();
    XLD(1, 0, 0); SB0();
    XLD(1, 1, 0); SB0();
    XLD(1, 0, 1); SB0();
    XLD(1, 1, 1); SB0();
    XLD(1, 0, 2); SB0();
    XLD(1, 1, 2); SB0();
    __builtin_amdgcn_s_setprio(0);
    SB0(); SETUP_P(); SETUP_M(); SETUP_Z(); SB0();
    VMW(6); SB0();
    asm volatile("" : XC_(fh[0][0]), XC_(fh[1][0])); SB0();
    STAMP(1);
    TDV(0); SB0();
    TMF(0, 0, 0); SB0();
    TMF(0, 1, 0); SB0();
    XLD(2, 0, 0); SB0();
    TMF(0, 0, 1); SB0();
    TMF(0, 1, 1); SB0();
    XLD(2, 1, 0); SB0();
    TMF(1, 0, 0); SB0();
    TCV(0, 0, 0); SB0();
    TCV(1, 0, 1); SB0();
    TM0(0);
    TWR(0, 0, 0, 0); SB0();
    TCV(2, 0, 2); SB0();
    TMF(1, 1, 0); SB0();
    XLD(2, 0, 1); SB0();
    TWR(1, 0, 0, 1); SB0();
    TCV(3, 0, 3); SB0();
    TWR(2, 0, 0, 2); SB0();
    TMF(1, 0, 1); SB0();
    TCV(0, 0, 4); SB0();
    TWR(3, 0, 0, 3); SB0();
    TCV(1, 0, 5); SB0();
    TWR(0, 0, 0, 4); SB0();
    TMF(1, 1, 1); SB0();
    XLD(2, 1, 1); SB0();
    TCV(2, 0, 6); SB0();
    TWR(1, 0, 0, 5); SB0();
    TWR(2, 0, 0, 6); SB0();
    TMF(2, 0, 0); SB0();
    TCV(3, 1, 0); SB0();
    TCV(0, 1, 1); SB0();
    TWR(3, 0, 1, 0); SB0();
    TCV(1, 1, 2); SB0();
    TMF(2, 1, 0); SB0();
    XLD(2, 0, 2); SB0();
    TWR(0, 0, 1, 1); SB0();
    TCV(2, 1, 3); SB0();
    TWR(1, 0, 1, 2); SB0();
    TMF(2, 0, 1); SB0();
    TCV(3, 1, 4); SB0();
    TWR(2, 0, 1, 3); SB0();
    TCV(0, 1, 5); SB0();
    TWR(3, 0, 1, 4); SB0();
    TMF(2, 1, 1); SB0();
    XLD(2, 1, 2); SB0();
    TCV(1, 1, 6); SB0();
    TWR(0, 0, 1, 5); SB0();
    TWR(1, 0, 1, 6); SB0();
    TMF(3, 0, 0); SB0();
    TCV(2, 2, 0); SB0();
    TCV(3, 2, 1); SB0();
    TWR(2, 0, 2, 0); SB0();
    TCV(0, 2, 2); SB0();
    TMF(3, 1, 0); SB0();
    TWR(3, 0, 2, 1); SB0();
    TCV(1, 2, 3); SB0();
    TWR(0, 0, 2, 2); SB0();
    TMF(3, 0, 1); SB0();
    WLD(wf[0], vL, ws[0] + 0 * WP, -2048); SB0();
    TCV(2, 2, 4); SB0();
    TWR(1, 0, 2, 3); SB0();
    TCV(3, 2, 5); SB0();
    TMF(3, 1, 1); SB0();
    TWR(2, 0, 2, 4); SB0();
    TCV(0, 2, 6); SB0();
    TWR(3, 0, 2, 5); SB0();
    TWR(0, 0, 2, 6); SB0();
    VMW(7); SB0();
    TDV(1); SB0();
    TMF(0, 0, 0); SB0();
    TCV(1, 3, 0); SB0();
    TCV(2, 3, 1); SB0();
    TWR(1, 0, 3, 0); SB0();
    TCV(3, 3, 2); SB0();
    TMF(0, 1, 0); SB0();
    WLD(wf[1], vL, ws[0] + 0 * WP, -1024); SB0();
    TWR(2, 0, 3, 1); SB0();
    TCV(0, 3, 3); SB0();
    TWR(3, 0, 3, 2); SB0();
    TMF(0, 0, 1); SB0();
    TCV(1, 3, 4); SB0();
    TWR(0, 0, 3, 3); SB0();
    TCV(2, 3, 5); SB0();
    TWR(1, 0, 3, 4); SB0();
    TMF(0, 1, 1); SB0();
    TCV(3, 3, 6); SB0();
    TWR(2, 0, 3, 5); SB0();
    TWR(3, 0, 3, 6); SB0();
    TMF(1, 0, 0); SB0();
    TABD(); SB0();
    TCV(0, 0, 0); SB0();
    TCV(1, 0, 1); SB0();
    TM0(1);
    TWR(0, 1, 0, 0); SB0();
    TMF(1, 1, 0); SB0();
    TCV(2, 0, 2); SB0();
    TWR(1, 1, 0, 1); SB0();
    TCV(3, 0, 3); SB0();
    TWR(2, 1, 0, 2); SB0();
    TMF(1, 0, 1); SB0();
    TCV(0, 0, 4); SB0();
    TWR(3, 1, 0, 3); SB0();
    TCV(1, 0, 5); SB0();
    TWR(0, 1, 0, 4); SB0();
    TMF(1, 1, 1); SB0();
    WLD(wf[2], vL, ws[1] + 0 * WP, -2048); SB0();
    TCV(2, 0, 6); SB0();
    TWR(1, 1, 0, 5); SB0();
    TWR(2, 1, 0, 6); SB0();
    asm volatile("s_waitcnt lgkmcnt(7)" ::: "memory");
    BAR(); SB0();
    STAMP(2);
    XRD(0, 0, 0, 0);
    XRD(0, 0, 1, 0);
    XRD(0, 0, 0, 1);
    XRD(0, 0, 1, 1);
    XRD(0, 1, 0, 0);
    XRD(0, 1, 1, 0);
    SB0();
    TMF(2, 0, 0); SB0();
    TCV(3, 1, 0); SB0();
    TCV(0, 1, 1); SB0();
    TWR(3, 1, 1, 0); SB0();
    TCV(1, 1, 2); SB0();
    TMF(2, 1, 0); SB0();
    TWR(0, 1, 1, 1); SB0();
    TCV(2, 1, 3); SB0();
    TWR(1, 1, 1, 2); SB0();
    TMF(2, 0, 1); SB0();
    WLD(wf[3], vL, ws[1] + 0 * WP, -1024); SB0();
    TCV(3, 1, 4); SB0();
    TWR(2, 1, 1, 3); SB0();
    TCV(0, 1, 5); SB0();
    TMF(2, 1, 1); SB0();
    TWR(3, 1, 1, 4); SB0();
    TCV(1, 1, 6); SB0();
    TWR(0, 1, 1, 5); SB0();
    TWR(1, 1, 1, 6); SB0();
    TMF(3, 0, 0); SB0();
    TCV(2, 2, 0); SB0();
    TCV(3, 2, 1); SB0();
    TWR(2, 1, 2, 0); SB0();
    TMF(3, 1, 0); SB0();
    WLD(wf[4], vL, ws[0] + 0 * WP, 0); SB0();
    TCV(0, 2, 2); SB0();
    TWR(3, 1, 2, 1); SB0();
    TMF(3, 0, 1); SB0();
    TCV(1, 2, 3); SB0();
    TWR(0, 1, 2, 2); SB0();
    TCV(2, 2, 4); SB0();
    TMF(3, 1, 1); SB0();
    TWR(1, 1, 2, 3); SB0();
    TCV(3, 2, 5); SB0();
    TWR(2, 1, 2, 4); SB0();
    WAITV(4, wf[0], wf[1]); SB0();
    asm volatile("s_waitcnt lgkmcnt(15)" : XC_(xt[0][0][0][0]), XC_(xt[0][0][1][0]) :: "memory"); SB0();
    MM(aP[0][0][0], wf[0], xt[0][0][0][0], Z4); SB0();
    WLD(wf[5], vL, ws[0] + 0 * WP, 1024); SB0();
    TCV(0, 2, 6); SB0();
    TWR(3, 1, 2, 5); SB0();
    MM(aQ[0][0][0], wf[1], xt[0][0][1][0], Z4); SB0();
    TWR(0, 1, 2, 6); SB0();
    TCV(1, 3, 0); SB0();
    TCV(2, 3, 1); SB0();
    MM(aI[0][0][0], wf[0], xt[0][0][1][0], Z4); SB0();
    TWR(1, 1, 3, 0); SB0();
    TCV(3, 3, 2); SB0();
    TWR(2, 1, 3, 1); SB0();
    MM(aI[0][0][0], wf[1], xt[0][0][0][0], aI[0][0][0]); SB0();
    WLD(wf[6], vL, ws[1] + 0 * WP, 0); SB0();
    TCV(0, 3, 3); SB0();
    TWR(3, 1, 3, 2); SB0();
    asm volatile("s_waitcnt lgkmcnt(15)" : XC_(xt[0][0][0][1]), XC_(xt[0][0][1][1]) :: "memory"); SB0();
    MM(aP[0][0][1], wf[0], xt[0][0][0][1], Z4); SB0();
    TCV(1, 3, 4); SB0();
    TWR(0, 1, 3, 3); SB0();
    TCV(2, 3, 5); SB0();
    MM(aQ[0][0][1], wf[1], xt[0][0][1][1], Z4); SB0();
    TWR(1, 1, 3, 4); SB0();
    TCV(3, 3, 6); SB0();
    TWR(2, 1, 3, 5); SB0();
    MM(aI[0][0][1], wf[0], xt[0][0][1][1], Z4); SB0();
    WLD(wf[7], vL, ws[1] + 0 * WP, 1024); SB0();
    TWR(3, 1, 3, 6); SB0();
    MM(aI[0][0][1], wf[1], xt[0][0][0][1], aI[0][0][1]); SB0();
    asm volatile("s_waitcnt lgkmcnt(0)" ::: "memory");
    BAR(); SB0();
    STAMP(3);
    XRD(1, 0, 0, 0);
    XRD(1, 0, 1, 0);
    XRD(1, 0, 0, 1);
    XRD(1, 0, 1, 1);
    XRD(1, 1, 0, 0);
    XRD(1, 1, 1, 0);
    SB0();
    VMW(9); SB0();
    TDV(2); SB0();
    TMF(0, 0, 0); SB0();
    TMF(0, 1, 0); SB0();
    WLD(wf[8], vL, ws[0] + 1 * WP, -2048); SB0();
    TMF(0, 0, 1); SB0();
    TMF(0, 1, 1); SB0();
    WAITV(5, wf[2], wf[3]); SB0();
    asm volatile("" : XC_(xt[0][1][0][0]), XC_(xt[0][1][1][0])); SB0();
    MM(aP[0][1][0], wf[2], xt[0][1][0][0], Z4); SB0();
    WLD(wf[9], vL, ws[0] + 1 * WP, -1024); SB0();
    TCV(0, 0, 0); SB0();
    TCV(1, 0, 1); SB0();
    MM(aQ[0][1][0], wf[3], xt[0][1][1][0], Z4); SB0();
    TM0(2);
    TWR(0, 2, 0, 0); SB0();
    TCV(2, 0, 2); SB0();
    TWR(1, 2, 0, 1); SB0();
    MM(aI[0][1][0], wf[2], xt[0][1][1][0], Z4); SB0();
    TCV(3, 0, 3); SB0();
    TWR(2, 2, 0, 2); SB0();
    TCV(0, 0, 4); SB0();
    MM(aI[0][1][0], wf[3], xt[0][1][0][0], aI[0][1][0]); SB0();
    WLD(wf[10], vL, ws[0] + 0 * WP, 2048); SB0();
    TWR(3, 2, 0, 3); SB0();
    TCV(1, 0, 5); SB0();
    TMF(1, 0, 0); SB0();
    TWR(0, 2, 0, 4); SB0();
    TCV(2, 0, 6); SB0();
    TWR(1, 2, 0, 5); SB0();
    TWR(2, 2, 0, 6); SB0();
    TMF(1, 1, 0); SB0();
    TMF(1, 0, 1); SB0();
    WLD(wf[11], vL, ws[0] + 0 * WP, 3072); SB0();
    TMF(1, 1, 1); SB0();
    TMF(2, 0, 0); SB0();
    TCV(3, 1, 0); SB0();
    TCV(0, 1, 1); SB0();
    TWR(3, 2, 1, 0); SB0();
    TMF(2, 1, 0); SB0();
    WLD(wf[12], vL, ws[1] + 0 * WP, 2048); SB0();
    TCV(1, 1, 2); SB0();
    TWR(0, 2, 1, 1); SB0();
    TMF(2, 0, 1); SB0();
    TCV(2, 1, 3); SB0();
    TWR(1, 2, 1, 2); SB0();
    TCV(3, 1, 4); SB0();
    TMF(2, 1, 1); SB0();
    TWR(2, 2, 1, 3); SB0();
    TCV(0, 1, 5); SB0();
    TWR(3, 2, 1, 4); SB0();
    WAITV(7, wf[4], wf[5]); SB0();
    asm volatile("s_waitcnt lgkmcnt(15)" : XC_(xt[1][0][0][0]), XC_(xt[1][0][1][0]) :: "memory"); SB0();
    MM(aP[0][0][0], wf[4], xt[1][0][0][0], aP[0][0][0]); SB0();
    WLD(wf[13], vL, ws[1] + 0 * WP, 3072); SB0();
    TCV(1, 1, 6); SB0();
    TWR(0, 2, 1, 5); SB0();
    MM(aQ[0][0][0], wf[5], xt[1][0][1][0], aQ[0][0][0]); SB0();
    TWR(1, 2, 1, 6); SB0();
    TCV(2, 2, 0); SB0();
    TCV(3, 2, 1); SB0();
    MM(aI[0][0][0], wf[4], xt[1][0][1][0], aI[0][0][0]); SB0();
    TWR(2, 2, 2, 0); SB0();
    TCV(0, 2, 2); SB0();
    TWR(3, 2, 2, 1); SB0();
    MM(aI[0][0][0], wf[5], xt[1][0][0][0], aI[0][0][0]); SB0();
    WLD(wf[14], vL, ws[0] + 1 * WP, 0); SB0();
    TCV(1, 2, 3); SB0();
    TWR(0, 2, 2, 2); SB0();
    TMF(3, 0, 0); SB0();
    TCV(2, 2, 4); SB0();
    TWR(1, 2, 2, 3); SB0();
    TCV(3, 2, 5); SB0();
    TMF(3, 1, 0); SB0();
    TWR(2, 2, 2, 4); SB0();
    TCV(0, 2, 6); SB0();
    TWR(3, 2, 2, 5); SB0();
    TMF(3, 0, 1); SB0();
    WLD(wf[15], vL, ws[0] + 1 * WP, 1024); SB0();
    TWR(0, 2, 2, 6); SB0();
    TMF(3, 1, 1); SB0();
    asm volatile("s_waitcnt lgkmcnt(15)" : XC_(xt[1][0][0][1]), XC_(xt[1][0][1][1]) :: "memory"); SB0();
    MM(aP[0][0][1], wf[4], xt[1][0][0][1], aP[0][0][1]); SB0();
    TCV(1, 3, 0); SB0();
    TCV(2, 3, 1); SB0();
    TWR(1, 2, 3, 0); SB0();
    MM(aQ[0][0][1], wf[5], xt[1][0][1][1], aQ[0][0][1]); SB0();
    WLD(wf[16], vL, ws[0] + 1 * WP, 2048); SB0();
    TCV(3, 3, 2); SB0();
    TWR(2, 2, 3, 1); SB0();
    MM(aI[0][0][1], wf[4], xt[1][0][1][1], aI[0][0][1]); SB0();
    TCV(0, 3, 3); SB0();
    TWR(3, 2, 3, 2); SB0();
    TCV(1, 3, 4); SB0();
    MM(aI[0][0][1], wf[5], xt[1][0][0][1], aI[0][0][1]); SB0();
    TWR(0, 2, 3, 3); SB0();
    TCV(2, 3, 5); SB0();
    TWR(1, 2, 3, 4); SB0();
    WAITV(9, wf[6], wf[7]); SB0();
    asm volatile("" : XC_(xt[1][1][0][0]), XC_(xt[1][1][1][0])); SB0();
    MM(aP[0][1][0], wf[6], xt[1][1][0][0], aP[0][1][0]); SB0();
    WLD(wf[17], vL, ws[0] + 1 * WP, 3072); SB0();
    TCV(3, 3, 6); SB0();
    TWR(2, 2, 3, 5); SB0();
    MM(aQ[0][1][0], wf[7], xt[1][1][1][0], aQ[0][1][0]); SB0();
    TWR(3, 2, 3, 6); SB0();
    MM(aI[0][1][0], wf[6], xt[1][1][1][0], aI[0][1][0]); SB0();
    MM(aI[0][1][0], wf[7], xt[1][1][0][0], aI[0][1][0]); SB0();
    WLD(wf[18], vL, ws[1] + 1 * WP, -2048); SB0();
    asm volatile("s_waitcnt lgkmcnt(0)" ::: "memory");
    BAR(); SB0();
    STAMP(4);
    XRD(2, 0, 0, 0);
    XRD(2, 0, 1, 0);
    XRD(2, 0, 0, 1);
    XRD(2, 0, 1, 1);
    XRD(2, 1, 0, 0);
    XRD(2, 1, 1, 0);
    SB0();
    WAITV(9, wf[8], wf[9]); SB0();
    MM(aP[1][0][0], wf[8], xt[0][0][0][0], Z4); SB0();
    MM(aQ[1][0][0], wf[9], xt[0][0][1][0], Z4); SB0();
    MM(aI[1][0][0], wf[8], xt[0][0][1][0], Z4); SB0();
    WLD(wf[19], vL, ws[1] + 1 * WP, -1024); SB0();
    MM(aI[1][0][0], wf[9], xt[0][0][0][0], aI[1][0][0]); SB0();
    MM(aP[1][0][1], wf[8], xt[0][0][0][1], Z4); SB0();
    MM(aQ[1][0][1], wf[9], xt[0][0][1][1], Z4); SB0();
    WLD(wf[20], vL, ws[1] + 1 * WP, 0); SB0();
    MM(aI[1][0][1], wf[8], xt[0][0][1][1], Z4); SB0();
    MM(aI[1][0][1], wf[9], xt[0][0][0][1], aI[1][0][1]); SB0();
    WAITV(9, wf[10], wf[11]); SB0();
    asm volatile("s_waitcnt lgkmcnt(4)" : XC_(xt[2][0][0][0]), XC_(xt[2][0][1][0]) :: "memory"); SB0();
    MM(aP[0][0][0], wf[10], xt[2][0][0][0], aP[0][0][0]); SB0();
    WLD(wf[21], vL, ws[1] + 1 * WP, 1024); SB0();
    MM(aQ[0][0][0], wf[11], xt[2][0][1][0], aQ[0][0][0]); SB0();
    MM(aI[0][0][0], wf[10], xt[2][0][1][0], aI[0][0][0]); SB0();
    MM(aI[0][0][0], wf[11], xt[2][0][0][0], aI[0][0][0]); SB0();
    WLD(wf[22], vL, ws[1] + 1 * WP, 2048); SB0();
    asm volatile("s_waitcnt lgkmcnt(2)" : XC_(xt[2][0][0][1]), XC_(xt[2][0][1][1]) :: "memory"); SB0();
    MM(aP[0][0][1], wf[10], xt[2][0][0][1], aP[0][0][1]); SB0();
    MM(aQ[0][0][1], wf[11], xt[2][0][1][1], aQ[0][0][1]); SB0();
    ZC(0, 0, 0, 0); SB0();
    MM(aI[0][0][1], wf[10], xt[2][0][1][1], aI[0][0][1]); SB0();
    WLD(wf[23], vL, ws[1] + 1 * WP, 3072); SB0();
    ZC(0, 0, 0, 1); SB0();
    MM(aI[0][0][1], wf[11], xt[2][0][0][1], aI[0][0][1]); SB0();
    ZC(0, 0, 0, 2); SB0();
    WAITV(10, wf[12], wf[13]); SB0();
    asm volatile("s_waitcnt lgkmcnt(0)" : XC_(xt[2][1][0][0]), XC_(xt[2][1][1][0]) :: "memory"); SB0();
    MM(aP[0][1][0], wf[12], xt[2][1][0][0], aP[0][1][0]); SB0();
    ZC(0, 0, 0, 3); SB0();
    ZW(0, 0, 0); SB0();
    MM(aQ[0][1][0], wf[13], xt[2][1][1][0], aQ[0][1][0]); SB0();
    WLD(wf[24], vL, ws[0] + 2 * WP, -2048); SB0();
    ZC(0, 0, 1, 0); SB0();
    MM(aI[0][1][0], wf[12], xt[2][1][1][0], aI[0][1][0]); SB0();
    ZC(0, 0, 1, 1); SB0();
    MM(aI[0][1][0], wf[13], xt[2][1][0][0], aI[0][1][0]); SB0();
    ZC(0, 0, 1, 2); SB0();
    WAITV(9, wf[14], wf[15]); SB0();
    MM(aP[1][0][0], wf[14], xt[1][0][0][0], aP[1][0][0]); SB0();
    WLD(wf[25], vL, ws[0] + 2 * WP, -1024); SB0();
    ZC(0, 0, 1, 3); SB0();
    MM(aQ[1][0][0], wf[15], xt[1][0][1][0], aQ[1][0][0]); SB0();
    ZW(0, 0, 1); SB0();
    ZC(0, 1, 0, 0); SB0();
    MM(aI[1][0][0], wf[14], xt[1][0][1][0], aI[1][0][0]); SB0();
    ZC(0, 1, 0, 1); SB0();
    MM(aI[1][0][0], wf[15], xt[1][0][0][0], aI[1][0][0]); SB0();
    WLD(wf[26], vL, ws[0] + 2 * WP, 0); SB0();
    ZC(0, 1, 0, 2); SB0();
    MM(aP[1][0][1], wf[14], xt[1][0][0][1], aP[1][0][1]); SB0();
    ZC(0, 1, 0, 3); SB0();
    ZW(0, 1, 0); SB0();
    MM(aQ[1][0][1], wf[15], xt[1][0][1][1], aQ[1][0][1]); SB0();
    MM(aI[1][0][1], wf[14], xt[1][0][1][1], aI[1][0][1]); SB0();
    WLD(wf[27], vL, ws[0] + 2 * WP, 1024); SB0();
    MM(aI[1][0][1], wf[15], xt[1][0][0][1], aI[1][0][1]); SB0();
    STAMP(12);
    LGW0(); BAR(); SB0();
    STAMP(13);
    TABI();
    ZRD(0, 0); ZRD(1, 2 * M_ZT); SB0();
    WAITV(10, wf[16], wf[17]); SB0();
    MM(aP[1][0][0], wf[16], xt[2][0][0][0], aP[1][0][0]); SB0();
    MM(aQ[1][0][0], wf[17], xt[2][0][1][0], aQ[1][0][0]); SB0();
    WLD(wf[28], vL, ws[0] + 2 * WP, 2048); SB0();
    MM(aI[1][0][0], wf[16], xt[2][0][1][0], aI[1][0][0]); SB0();
    MM(aI[1][0][0], wf[17], xt[2][0][0][0], aI[1][0][0]); SB0();
    ZIW(0, 4); SB0();
    asm volatile("" : XC_(fi[0])); SB0();
    ZIM(0, 0); SB0();
    WLD(wf[29], vL, ws[0] + 2 * WP, 3072); SB0();
    ZIM(0, 1); SB0();
    ZC(1, 0, 0, 0); SB0();
    ZIW(1, 0); SB0();
    ZIM(1, 0); SB0();
    ZC(1, 0, 0, 1); SB0();
    ZIM(1, 1); SB0();
    WLD(wf[30], vL, ws[1] + 2 * WP, -2048); SB0();
    ZC(1, 0, 0, 2); SB0();
    MM(aP[1][0][1], wf[16], xt[2][0][0][1], aP[1][0][1]); SB0();
    MM(aQ[1][0][1], wf[17], xt[2][0][1][1], aQ[1][0][1]); SB0();
    ZIC(0, 0); SB0();
    MM(aI[1][0][1], wf[16], xt[2][0][1][1], aI[1][0][1]); SB0();
    WLD(wf[31], vL, ws[1] + 2 * WP, -1024); SB0();
    ZIC(0, 1); SB0();
    MM(aI[1][0][1], wf[17], xt[2][0][0][1], aI[1][0][1]); SB0();
    ZIC(0, 2); SB0();
    WAITV(12, wf[18], wf[19]); SB0();
    MM(aP[1][1][0], wf[18], xt[0][1][0][0], Z4); SB0();
    YST(zo, ys + 0 * YT + 0 * YG); SB0();
    ZIC(1, 0); SB0();
    MM(aQ[1][1][0], wf[19], xt[0][1][1][0], Z4); SB0();
    WLD(wf[32], vL, ws[1] + 2 * WP, 0); SB0();
    ZIC(1, 1); SB0();
    MM(aI[1][1][0], wf[18], xt[0][1][1][0], Z4); SB0();
    ZIC(1, 2); SB0();
    MM(aI[1][1][0], wf[19], xt[0][1][0][0], aI[1][1][0]); SB0();
    YST(zo, ys + 2 * YT + 0 * YG); SB0();
    ZC(1, 0, 0, 3); SB0();
    WAITV(13, wf[20], wf[21]); SB0();
    MM(aP[1][1][0], wf[20], xt[1][1][0][0], aP[1][1][0]); SB0();
    WLD(wf[33], vL, ws[1] + 2 * WP, 1024); SB0();
    ZW(1, 0, 0); SB0();
    MM(aQ[1][1][0], wf[21], xt[1][1][1][0], aQ[1][1][0]); SB0();
    ZC(1, 0, 1, 0); SB0();
    MM(aI[1][1][0], wf[20], xt[1][1][1][0], aI[1][1][0]); SB0();
    ZC(1, 0, 1, 1); SB0();
    MM(aI[1][1][0], wf[21], xt[1][1][0][0], aI[1][1][0]); SB0();
    WLD(wf[34], vL, ws[1] + 2 * WP, 2048); SB0();
    ZC(1, 0, 1, 2); SB0();
    WAITV(13, wf[22], wf[23]); SB0();
    MM(aP[1][1][0], wf[22], xt[2][1][0][0], aP[1][1][0]); SB0();
    ZC(1, 0, 1, 3); SB0();
    ZW(1, 0, 1); SB0();
    MM(aQ[1][1][0], wf[23], xt[2][1][1][0], aQ[1][1][0]); SB0();
    MM(aI[1][1][0], wf[22], xt[2][1][1][0], aI[1][1][0]); SB0();
    WLD(wf[35], vL, ws[1] + 2 * WP, 3072); SB0();
    MM(aI[1][1][0], wf[23], xt[2][1][0][0], aI[1][1][0]); SB0();
    WAITV(12, wf[24], wf[25]); SB0();
    MM(aP[2][0][0], wf[24], xt[0][0][0][0], Z4); SB0();
    MM(aQ[2][0][0], wf[25], xt[0][0][1][0], Z4); SB0();
    ZC(1, 1, 0, 0); SB0();
    MM(aI[2][0][0], wf[24], xt[0][0][1][0], Z4); SB0();
    ZC(1, 1, 0, 1); SB0();
    MM(aI[2][0][0], wf[25], xt[0][0][0][0], aI[2][0][0]); SB0();
    ZC(1, 1, 0, 2); SB0();
    MM(aP[2][0][1], wf[24], xt[0][0][0][1], Z4); SB0();
    ZC(1, 1, 0, 3); SB0();
    ZW(1, 1, 0); SB0();
    MM(aQ[2][0][1], wf[25], xt[0][0][1][1], Z4); SB0();
    MM(aI[2][0][1], wf[24], xt[0][0][1][1], Z4); SB0();
    MM(aI[2][0][1], wf[25], xt[0][0][0][1], aI[2][0][1]); SB0();
    STAMP(14);
    LGW0(); BAR(); SB0();
    STAMP(15);
    ZRD(0, M_Z1 + 0); ZRD(1, M_Z1 + 2 * M_ZT); SB0();
    WAITV(10, wf[26], wf[27]); SB0();
    MM(aP[2][0][0], wf[26], xt[1][0][0][0], aP[2][0][0]); SB0();
    MM(aQ[2][0][0], wf[27], xt[1][0][1][0], aQ[2][0][0]); SB0();
    MM(aI[2][0][0], wf[26], xt[1][0][1][0], aI[2][0][0]); SB0();
    MM(aI[2][0][0], wf[27], xt[1][0][0][0], aI[2][0][0]); SB0();
    ZIW(0, 4); SB0();
    ZIM(0, 0); SB0();
    ZIM(0, 1); SB0();
    ZIW(1, 0); SB0();
    ZIM(1, 0); SB0();
    ZIM(1, 1); SB0();
    MM(aP[2][0][1], wf[26], xt[1][0][0][1], aP[2][0][1]); SB0();
    MM(aQ[2][0][1], wf[27], xt[1][0][1][1], aQ[2][0][1]); SB0();
    ZIC(0, 0); SB0();
    MM(aI[2][0][1], wf[26], xt[1][0][1][1], aI[2][0][1]); SB0();
    ZIC(0, 1); SB0();
    MM(aI[2][0][1], wf[27], xt[1][0][0][1], aI[2][0][1]); SB0();
    ZIC(0, 2); SB0();
    WAITV(8, wf[28], wf[29]); SB0();
    MM(aP[2][0][0], wf[28], xt[2][0][0][0], aP[2][0][0]); SB0();
    YST(zo, ys + 0 * YT + 1 * YG); SB0();
    ZIC(1, 0); SB0();
    MM(aQ[2][0][0], wf[29], xt[2][0][1][0], aQ[2][0][0]); SB0();
    ZIC(1, 1); SB0();
    MM(aI[2][0][0], wf[28], xt[2][0][1][0], aI[2][0][0]); SB0();
    ZIC(1, 2); SB0();
    MM(aI[2][0][0], wf[29], xt[2][0][0][0], aI[2][0][0]); SB0();
    YST(zo, ys + 2 * YT + 1 * YG); SB0();
    MM(aP[2][0][1], wf[28], xt[2][0][0][1], aP[2][0][1]); SB0();
    MM(aQ[2][0][1], wf[29], xt[2][0][1][1], aQ[2][0][1]); SB0();
    ZC(2, 0, 0, 0); SB0();
    MM(aI[2][0][1], wf[28], xt[2][0][1][1], aI[2][0][1]); SB0();
    ZC(2, 0, 0, 1); SB0();
    MM(aI[2][0][1], wf[29], xt[2][0][0][1], aI[2][0][1]); SB0();
    ZC(2, 0, 0, 2); SB0();
    WAITV(8, wf[30], wf[31]); SB0();
    MM(aP[2][1][0], wf[30], xt[0][1][0][0], Z4); SB0();
    ZC(2, 0, 0, 3); SB0();
    ZW(2, 0, 0); SB0();
    MM(aQ[2][1][0], wf[31], xt[0][1][1][0], Z4); SB0();
    ZC(2, 0, 1, 0); SB0();
    MM(aI[2][1][0], wf[30], xt[0][1][1][0], Z4); SB0();
    ZC(2, 0, 1, 1); SB0();
    MM(aI[2][1][0], wf[31], xt[0][1][0][0], aI[2][1][0]); SB0();
    ZC(2, 0, 1, 2); SB0();
    WAITV(4, wf[32], wf[33]); SB0();
    MM(aP[2][1][0], wf[32], xt[1][1][0][0], aP[2][1][0]); SB0();
    ZC(2, 0, 1, 3); SB0();
    ZW(2, 0, 1); SB0();
    MM(aQ[2][1][0], wf[33], xt[1][1][1][0], aQ[2][1][0]); SB0();
    MM(aI[2][1][0], wf[32], xt[1][1][1][0], aI[2][1][0]); SB0();
    MM(aI[2][1][0], wf[33], xt[1][1][0][0], aI[2][1][0]); SB0();
    WAITV(2, wf[34], wf[35]); SB0();
    MM(aP[2][1][0], wf[34], xt[2][1][0][0], aP[2][1][0]); SB0();
    MM(aQ[2][1][0], wf[35], xt[2][1][1][0], aQ[2][1][0]); SB0();
    MM(aI[2][1][0], wf[34], xt[2][1][1][0], aI[2][1][0]); SB0();
    MM(aI[2][1][0], wf[35], xt[2][1][0][0], aI[2][1][0]); SB0();
    STAMP(5);
    ZC(2, 1, 0, 0); SB0();
    ZC(2, 1, 0, 1); SB0();
    ZC(2, 1, 0, 2); SB0();
    ZC(2, 1, 0, 3); SB0();
    ZW(2, 1, 0); SB0();
    VMW(0); ARRIVE(flp + 0 * CN_P); SB0();
    LGW0(); BAR(); SB0();
    }
  }
  STAMP(6);
  ZRD(0, 2 * M_Z1); ZRD(1, 2 * M_Z1 + 2 * M_ZT);
  asm volatile("" : "+v"(vL));
  lane = vL >> 4; g = lane >> 4; r = lane & 15; q = (lane >> 2) & 3; p = lane & 3;
  ZINV2(2);
  if (L >= I_NWG) { VMW(0); ARRIVE(flp + CN_P); STAMP_END(11); return; }
  POLL(1);
  TABG();
  asm volatile("s_waitcnt vmcnt(0) lgkmcnt(0)" : "+v"(gi[0][0]), "+v"(gi[0][1]), "+v"(gi[1][0]), "+v"(gi[1][1]) :: "memory");
  ARRIVE(flp + CN_P); CHECK(1, 1);

  const int ifw = 2 * g + (q >> 1);
  const unsigned ia = sbase + M_RA + (p >> 1) * 512 + (q & 1) * 32 + (p & 1) * 16;
  const unsigned i0 = ia + ifw * 1024 + ((wave ^ (ifw & 3)) << 6), is = ia + 16 * 1024 + (wave << 6);
  const rsrc_t rs = make_rsrc(KPTR(1), IMG * IMG * CH * 2);
  const int tq = ((L >> 2) * 37) >> 8;
  const int px0 = (32 * ((L >> 2) - 7 * tq) + 8 * (L & 3) + wave) * IMG + 28 * tq;
  const unsigned vo = (r < 14) ? (unsigned)((px0 + r) * (CH * 2) + g * 16) : OOB;
  constexpr unsigned C14H = split_hl(1.0 / 14.0, 0), C14L = split_hl(1.0 / 14.0, 1);
  const u32x4 se[2] = {u32x4{0u, 0u, (lane == 0) ? C14H : 0u, (lane == 0) ? C14L : 0u}, u32x4{0u, 0u, (lane == 13) ? C14H << 16 : 0u, (lane == 13) ? C14L << 16 : 0u}};
  u32x2 id[2][2][3];
  u32x4 io[2][2];
#define IRD(s) do { _Pragma("unroll") for (int t_ = 0; t_ < 2; ++t_) { \
    TRR(id[(s) & 1][t_][0], i0, (s) * YS + t_ * 8); TRR(id[(s) & 1][t_][1], i0, (s) * YS + t_ * 8 + 8192); TRR(id[(s) & 1][t_][2], is, (s) * YS + t_ * 8); } } while (0)
#define IINV(s, n) do { u32x2 (&d_)[2][3] = id[(s) & 1]; \
    asm volatile("s_waitcnt lgkmcnt(%0)" :: "i"(n) : "memory"); SB0(); \
    u32x6 v_[2] = {u32x6{d_[0][0][0], d_[0][0][1], d_[0][1][0], d_[0][1][1], d_[0][2][0], d_[0][2][1]}, \
                   u32x6{d_[1][0][0], d_[1][0][1], d_[1][1][0], d_[1][1][1], d_[1][2][0], d_[1][2][1]}}; \
    asm volatile("" : "+v"(v_[0]), "+v"(v_[1])); \
    _Pragma("unroll") for (int pt = 0; pt < 2; ++pt) { f32x4 e_[2]; \
      _Pragma("unroll") for (int t_ = 0; t_ < 2; ++t_) { const u32x4 a_ = __builtin_shufflevector(v_[t_], v_[t_], 0, 1, 2, 3); \
        e_[t_] = mfma32(a_, gi[pt][0], mfma32(a_, gi[pt][1], mfma32(__builtin_shufflevector(v_[t_], v_[t_], 2, 3, 4, 5), se[pt], Z4))); } \
      io[(s) & 1][pt] = pack8(e_[0], e_[1]); } } while (0)
#define IST(s) do { __builtin_amdgcn_raw_buffer_store_b128(io[(s) & 1][0], rs, vo + (s) * 64, 0, ST_AUX); \
    __builtin_amdgcn_raw_buffer_store_b128(io[(s) & 1][1], rs, vo + (s) * 64 + 14 * CH * 2, 0, ST_AUX); } while (0)
  STAMP(7);
  BAR();
  IDMA(0); IDMA(1);
  POLL(2); CHECK(0, 2);
  STAMP(8);
  BAR();
  IDMA(2);
  IRD(0); IRD(1);
  IINV(0, 6); IINV(1, 0);
  {
    const unsigned va = (unsigned)((px0 + (r & 7)) * (CH * 2) + g * 16 + (r >> 3) * 64), vb = ((r & 7) < 6) ? va + 8 * CH * 2 : OOB;
#pragma unroll
    for (int pt = 0; pt < 2; ++pt) {
      u32x4 sa, sb;
#pragma unroll
      for (int e = 0; e < 4; ++e) {
        sa[e] = __builtin_amdgcn_update_dpp(io[0][pt][e], io[1][pt][e], 0x128, 0xf, 0xc, false);
        sb[e] = __builtin_amdgcn_update_dpp(io[1][pt][e], io[0][pt][e], 0x128, 0xf, 0x3, false);
      }
      __builtin_amdgcn_raw_buffer_store_b128(sa, rs, va + pt * (14 * CH * 2), 0, ST_AUX);
      __builtin_amdgcn_raw_buffer_store_b128(sb, rs, vb + pt * (14 * CH * 2), 0, ST_AUX);
    }
  }
  VMW(4);
  STAMP(9);
  BAR();
  IRD(2);
  IINV(2, 0); IST(2);
  asm volatile("fftconv_code_end:");
  STAMP_END(11);
}

extern "C" void kernel_launch(void* const* d_in, const int* in_sizes, int n_in,
                              void* d_out, int out_size, void* d_ws, size_t ws_size,
                              hipStream_t stream) {
  const char* x = (const char*)d_in[0];
  const char* w = (const char*)d_in[1];
  char* ws = (char*)d_ws;
  k_fwd<<<dim3(F_NWG), dim3(512), 0, stream>>>(x, w, ws, (char*)d_out INSTR_ARG(0));
  k_mix<<<dim3(M_NWG), dim3(512), 0, stream>>>(ws, (char*)d_out INSTR_ARG(1));
}
```

```cpp
#include <hip/hip_runtime.h>
#include <hip/hip_bf16.h>

typedef __bf16 bf16x8 __attribute__((ext_vector_type(8)));
typedef __bf16 bf16x2 __attribute__((ext_vector_type(2)));
typedef float  f32x4  __attribute__((ext_vector_type(4)));
typedef unsigned u32x2 __attribute__((ext_vector_type(2)));
typedef unsigned u32x4 __attribute__((ext_vector_type(4)));
typedef unsigned u32x6 __attribute__((ext_vector_type(6)));
typedef unsigned u32x8 __attribute__((ext_vector_type(8)));
typedef unsigned u32x10 __attribute__((ext_vector_type(10)));

#define LDS3 __attribute__((address_space(3)))

#define INSTR_PARAM
#define INSTR_ARG(k)
#define INSTR_DECL
#define STAMP(i)
#define CYC0()
#define CYC1()
#define STAMP_END(i)
#define STAMP_MID()
#define INSTR_LDS 0

constexpr int IMG = 224, CH = 96;
constexpr int NFW = 17;

constexpr int    XT = 224 * 64, XC = 8 * XT, XF = 2 * XC;
constexpr size_t XW_OFF = (size_t)36 << 20;
constexpr int    XC2 = 2 * XC, XF2 = 1 << 20;
constexpr size_t WT_OFF = 0;
constexpr int    WK = 16384, WP = 14 * WK, WH = 1 << 20, WF = 2 * WH;
constexpr size_t YH_OFF = (size_t)96 << 20;
constexpr int    YS = NFW * 1024, YG = 3 * 16384, YT = 3 * YG, YQ = 1 << 20;
constexpr size_t CN_OFF = (size_t)34 << 20;
constexpr int    FL_W = 896 * 128, FL_Y = FL_W + 288 * 128;
constexpr int    CN_G = 384, CN_P = 56 * CN_G, FL_V = FL_Y + 3 * CN_P;
constexpr size_t EP_OFF = (size_t)32 << 20 | 0xc0000;
constexpr unsigned long long EP_STEP = 0x9e3779b97f4a7c15ull;

struct CosTab { double v[448]; };
constexpr CosTab make_cos() {
  CosTab t{};
  const double pi = 3.14159265358979323846264338327950288;
  for (int k = 0; k <= 112; ++k) {
    const double a = 2.0 * pi * k / 448.0;
    double term = 1.0, sum = 1.0;
    for (int n = 1; n <= 14; ++n) { term *= -a * a / ((2.0 * n - 1.0) * (2.0 * n)); sum += term; }
    if (k == 112) sum = 0.0;
    t.v[k] = sum; t.v[(448 - k) % 448] = sum; t.v[224 - k] = -sum; t.v[224 + k] = -sum;
  }
  return t;
}
constexpr CosTab kCos = make_cos();
constexpr double cs(int idx) { return kCos.v[(idx + 448) % 448]; }
constexpr unsigned short bf16_rne(float f) {
  unsigned u = __builtin_bit_cast(unsigned, f);
  u += 0x7fffu + ((u >> 16) & 1u);
  return (unsigned short)(u >> 16);
}
constexpr float bf16_val(unsigned short b) { return __builtin_bit_cast(float, (unsigned)b << 16); }
constexpr unsigned short split_hl(double v, int hl) {
  const unsigned short hi = bf16_rne((float)v);
  return hl == 0 ? hi : bf16_rne((float)(v - (double)bf16_val(hi)));
}
struct FragD { unsigned short v[2][2][64][8]; };
struct TabM { unsigned short fh[2][2][2][64][8], fi[2][2][64][8], g[2][2][64][8]; FragD d; };
constexpr FragD make_D() {
  FragD t{};
  for (int ct = 0; ct < 2; ++ct) for (int l = 0; l < 64; ++l) for (int jj = 0; jj < 8; ++jj) {
    const int cp = 16 * ct + (l & 15), v = 8 * (l >> 4) + jj;
    const double e = cs(7 * (2 * (cp >> 1) + 1) * v + 112 * (cp & 1));
    for (int hl = 0; hl < 2; ++hl) t.v[ct][hl][l][jj] = split_hl(e, hl);
  }
  return t;
}
constexpr TabM make_M() {
  TabM t{};
  t.d = make_D();
  for (int s = 0; s < 2; ++s) for (int l = 0; l < 64; ++l) for (int jj = 0; jj < 8; ++jj) {
    const int n = l & 15, g = l >> 4, hh = jj >> 2, q = jj & 3;
    for (int ct = 0; ct < 2; ++ct) {
      const int kr = 16 * hh + 4 * g + q, a = kr >> 1, ri = kr & 1, cp = 4 * (4 * ct + (n & 3)) + (n >> 2), k = cp >> 1, ro = cp & 1;
      double e = 0.0;
      if (a < 14 && k < 14) e = (s == 0) ? cs(32 * k * a + 112 * (ro - ri)) : (ri == k / 7) ? cs(16 * (2 * (k % 7) + 1) * a + 112 * ro) : 0.0;
      for (int hl = 0; hl < 2; ++hl) t.fh[s][ct][hl][l][jj] = split_hl(e, hl);
    }
    const int k = 8 * hh + 2 * g + (q >> 1), ri = q & 1;
    {
      const int j = n >> 1, ro = n & 1;
      double e = 0.0;
      if (k < 14) e = (s == 0) ? cs(32 * k * (j + 6) + 112 * (ri - ro)) : (ro == k / 7) ? 2.0 * cs(16 * (2 * (k % 7) + 1) * (j + 6) + 112 * ri) : 0.0;
      for (int hl = 0; hl < 2; ++hl) t.fi[s][hl][l][jj] = split_hl(e, hl);
    }
    {
      const double e = (n < 14) ? cs(7 * (2 * k + 1) * (14 * s + n + 5) + 112 * ri) / 224.0 : 0.0;
      for (int hl = 0; hl < 2; ++hl) t.g[s][hl][l][jj] = split_hl(e, hl);
    }
  }
  return t;
}
__device__ __attribute__((aligned(4096))) char gXh[NFW * XF];
__device__ __attribute__((aligned(64))) const TabM kTabM = make_M();

__device__ __forceinline__ unsigned pack2(float a, float b) {
  bf16x2 v; v[0] = (__bf16)a; v[1] = (__bf16)b;
  return __builtin_bit_cast(unsigned, v);
}
__device__ __forceinline__ f32x4 mfma32(u32x4 a, u32x4 b, f32x4 c) {
  return __builtin_amdgcn_mfma_f32_16x16x32_bf16(__builtin_bit_cast(bf16x8, a), __builtin_bit_cast(bf16x8, b), c, 0, 0, 0);
}
__device__ __forceinline__ u32x4 cat(u32x2 a, u32x2 b) { return u32x4{a[0], a[1], b[0], b[1]}; }
__device__ __forceinline__ u32x4 pack8(f32x4 a, f32x4 b) { return u32x4{pack2(a[0], a[1]), pack2(a[2], a[3]), pack2(b[0], b[1]), pack2(b[2], b[3])}; }
#define Z4 (f32x4{0.f, 0.f, 0.f, 0.f})
#define ST_AUX 16
typedef __amdgpu_buffer_rsrc_t rsrc_t;
__device__ __forceinline__ rsrc_t make_rsrc(void* p, unsigned bytes) { return __builtin_amdgcn_make_buffer_rsrc(p, 0, bytes, 0x00020000); }
constexpr unsigned OOB = 0x80000000u;
#define SB0() __builtin_amdgcn_sched_barrier(0)
#define BAR() __builtin_amdgcn_s_barrier()
#define VMW(n) asm volatile("s_waitcnt vmcnt(%0)" :: "i"(n) : "memory")
#define TRR(dst, addr, off) asm volatile("ds_read_b64_tr_b16 %0, %1 offset:%2" : "=v"(dst) : "v"(addr), "i"(off) : "memory")
#define GLD(dst, voff, sb, imm) asm volatile("global_load_dwordx4 %0, %1, %2 offset:%3" : "=v"(dst) : "v"(voff), "s"(sb), "i"(imm) : "memory")
#define LD_SC " sc1"
#define GLD1(dst, voff, sb, imm) asm volatile("global_load_dwordx4 %0, %1, %2 offset:%3" LD_SC : "=v"(dst) : "v"(voff), "s"(sb), "i"(imm) : "memory")
#define DMA1(m0v, voff, sb, imm) asm volatile("s_mov_b32 m0, %0\n\ts_nop 0\n\tglobal_load_lds_dwordx4 %1, %2 offset:%3 sc1" :: "s"(m0v), "v"(voff), "s"(sb), "i"(imm) : "memory")
#define DMA(m0v, voff, sb, imm) asm volatile("s_mov_b32 m0, %0\n\ts_nop 0\n\tglobal_load_lds_dwordx4 %1, %2 offset:%3" :: "s"(m0v), "v"(voff), "s"(sb), "i"(imm) : "memory")
#define BST(v, voff, rs, so) asm volatile("buffer_store_dwordx4 %0, %1, %2, %3 offen sc1\n\ts_nop 1" :: "v"(v), "v"(voff), "s"(rs), "s"(so) : "memory")
#define WAIT2(n, a, b) asm volatile("s_waitcnt lgkmcnt(%2)" : "+v"(a), "+v"(b) : "i"(n) : "memory")
#define WAIT4(n, a, b, c, d) asm volatile("s_waitcnt lgkmcnt(%4)" : "+v"(a), "+v"(b), "+v"(c), "+v"(d) : "i"(n) : "memory")

#define TWL 0
#define XPR 2
#define WG_X 200
#define WG_W 200
constexpr int W_GATE = WG_X;
constexpr int W_SLOPE = 4, W_OFF = 290;
constexpr int F_RS = 768 * 16;
constexpr int F_WOFF = 4 * F_RS;
constexpr int F_CNT = 154112;
constexpr int F_NW0 = 32;
constexpr int F_NPF = 16;
constexpr int F_NWG = 256;

__device__ __forceinline__ int comp_of(int u, int n) { return n < 56 ? 56 * u + n : n < 60 ? 392 + 4 * u + n : 476; }
__device__ __forceinline__ void make_e(int cp, int g, u32x4 (&e)[2][2]) {
  const int fw = (cp * 2341) >> 16, rem = cp - 28 * fw, k = rem >> 1;
  const bool sp = fw >= 16;
  const float fa = sp ? 0.f : (float)(2 * fw + 1) * (1.f / 64.f);
  const float fb = (float)(sp ? 2 * (k >= 7 ? k - 7 : k) + 1 : 2 * k) * (1.f / 28.f);
#pragma unroll
  for (int ks = 0; ks < 2; ++ks) {
    const float f0 = fb * (float)(4 * ks + g) + ((rem & 1) ? 0.25f : 0.f);
    const bool ok = (ks == 0) || g < 3;
    const float m0 = (ok && (!sp || k >= 7)) ? 1.f : 0.f, m1 = (ok && !sp) ? 1.f : 0.f, m6 = (ok && (!sp || k < 7)) ? 1.f : 0.f;
    float v[8];
#pragma unroll
    for (int j = 0; j < 7; ++j) v[j] = __builtin_amdgcn_cosf(__builtin_fmaf(fa, (float)j, f0)) * (j == 0 ? m0 : j == 6 ? m6 : m1);
    v[7] = 0.f;
#pragma unroll
    for (int s = 0; s < 4; ++s) {
      const unsigned h = pack2(v[2 * s], v[2 * s + 1]);
      e[ks][0][s] = h;
      e[ks][1][s] = pack2(v[2 * s] - __builtin_bit_cast(float, h << 16), v[2 * s + 1] - __builtin_bit_cast(float, h & 0xffff0000u));
    }
  }
}

#define GATE(lim, slope, off) do { unsigned lim_; \
    asm volatile("s_memrealtime vcc\n\ts_waitcnt lgkmcnt(0)\n\ts_sub_u32 vcc_lo, vcc_lo, %1\n\ts_mul_i32 %0, vcc_lo, %3\n\ts_sub_i32 %0, %0, %4\n\ts_max_i32 %0, %0, 0\n\ts_min_u32 %0, %0, %2\n" \
                 "1:\n\ts_cmp_ge_u32 vcc_lo, %0\n\ts_cbranch_scc1 2f\n\ts_memrealtime vcc\n\ts_waitcnt lgkmcnt(0)\n\ts_sub_u32 vcc_lo, vcc_lo, %1\n\ts_branch 1b\n2:" \
                 : "=&s"(lim_) : "s"((unsigned)t0), "i"(lim), "i"(slope), "i"(off) : "memory", "vcc", "scc"); } while (0)
#define KPTR(i) ((char*)(((unsigned long long)ka[2 * (i) + 1] << 32) | ka[2 * (i)]))
#define WSP(i) ((char*)(((unsigned long long)KPTR(i) + 0x3fffffu) & ~0x3fffffull))
#define PLD() asm volatile("global_load_dwordx2 %0, %1, %2 offset:128 sc1" : "=v"(pw) : "v"((lane & 7) * 8), "s"(ep) : "memory")
#define PLC(n) do { unsigned st_; asm volatile("s_mov_b32 %[sp], 0\n\ts_waitcnt vmcnt(%[nn])\n" \
    "1:\n\tv_lshrrev_b64 %[t], 16, %[v]\n\tv_cmp_ne_u64 vcc, %[tg], %[t]\n\ts_nop 4\n\ts_cbranch_vccz 2f\n\t" \
    "s_add_u32 %[sp], %[sp], 1\n\ts_cmp_ge_u32 %[sp], %[lim]\n\ts_cbranch_scc1 2f\n\ts_sleep 2\n\t" \
    "global_load_dwordx2 %[v], %[z], %[p] offset:128 sc1\n\ts_waitcnt vmcnt(0)\n\ts_branch 1b\n2:" \
    : [v] "+v"(pw), [sp] "=&s"(st_), [t] "=&v"(pt) : [z] "v"((lane & 7) * 8), [p] "s"(ep), [tg] "s"(tag >> 16), [lim] "i"(SPIN_MAX), [nn] "i"(n) : "memory", "scc", "vcc"); \
    unsigned sm_ = pw[0] + __builtin_amdgcn_update_dpp(0u, pw[0], 0xb1, 0xf, 0xf, true); \
    sm_ += __builtin_amdgcn_update_dpp(0u, sm_, 0x4e, 0xf, 0xf, true); sm_ += __builtin_amdgcn_update_dpp(0u, sm_, 0x141, 0xf, 0xf, true); \
    pl = __builtin_amdgcn_readfirstlane(((pw[0] >> 12) & 7u) | ((~sm_ >> 8) & 8u)); } while (0)
#define PMASK(pl_) ((0xf0f0u >> ((pl_) & 7)) ^ (0u - (((pl_) >> 3) & 1)))
constexpr int SPIN_MAX = 1 << 16;
#define LANDED(sh) do { unsigned n_; \
    asm volatile("s_waitcnt vmcnt(0)\n\ts_mov_b64 exec, %3\n\tds_add_u32 %1, %2\n\ts_mov_b64 exec, -1\n" \
                 "1:\n\tds_read_u8 %0, %1 offset:%4\n\ts_waitcnt lgkmcnt(0)\n\tv_cmp_gt_u32 vcc, 4, %0\n\ts_nop 4\n\ts_cbranch_vccnz 1b" \
                 : "=&v"(n_) : "v"(sbase + F_CNT), "v"(1u << (8 * (sh))), "s"((sh) ? (unsigned long long)(wave >> 2) : 1ull), "i"(sh) : "memory", "vcc"); SB0(); } while (0)
#define WDMA() do { if (wave >= 4) { const char* wgt = KPTR(1) + oW; \
    _Pragma("unroll") for (int i = 0; i < 7; ++i) DMA(sbase + F_WOFF + w4 * 1024 + i * 4096, wv[i & 1], wgt + i * (7 * CH * CH * 2), 0); } } while (0)
#define KWAIT() do { SB0(); asm volatile("s_waitcnt lgkmcnt(0)\n\ts_memrealtime %0" : "=s"(t0) :: "memory"); SB0(); asm volatile("" : "+s"(ka), "+s"(s3), "+s"(s4)); } while (0)

constexpr int M_XT = 1824;
constexpr int M_RA = 0, M_RB = 32 * M_XT;
constexpr int M_ZT = 912, M_Z1 = 32 * M_ZT;
constexpr int M_TAB = M_RB + 3 * M_Z1;
constexpr int M_LDS = F_CNT + 512 + INSTR_LDS;
static_assert(F_CNT == M_TAB + 8192, "");
constexpr int M_NI = NFW * 14, I_NWG = 224;
constexpr int POLL_SLEEP = 2;
#define H_LO 0

#define DSR(dst, addr, off) asm volatile("ds_read_b128 %0, %1 offset:%2" : "=v"(dst) : "v"(addr), "i"(off) : "memory")
#define DSW(addr, v, off) asm volatile("ds_write_b128 %0, %1 offset:%2" :: "v"(addr), "v"(v), "i"(off) : "memory")
#define WLD GLD1
#define XC_(r) "+v"(r)
#define PIN(r) asm volatile("" :: "v"(r))
#define MM(d, a, b, c) do { d = mfma32(a, b, c); PIN(d); } while (0)
#define WAITV(n, a, b) asm volatile("s_waitcnt vmcnt(%2)" : "+v"(a), "+v"(b) : "i"(n) : "memory")
#define LGW0() asm volatile("s_waitcnt lgkmcnt(0)" ::: "memory")
#define XLD(cc, chg, i) do { if ((i) < 2) GLD1(xr[cc][chg][(i) & 1], vL, (cc) ? xs2 + (2 * (cc) + (chg) - 2) * XC2 : xs + (chg) * XC, (i) * 1024); \
    else asm volatile("global_load_dwordx2 %0, %1, %2" LD_SC : "=v"(xh[cc][chg]) : "v"(vL), "s"((cc) ? xsh2 + (2 * (cc) + (chg) - 2) * XC2 : xsh + (chg) * XC) : "memory"); } while (0)
#define ARRIVE(p) asm volatile("s_mov_b64 exec, 1\n\tglobal_store_dwordx2 %0, %1, %2 sc1\n\ts_mov_b64 exec, -1" :: "v"(0u), "v"(tag), "s"(p) : "memory")
#define ARRIVEL(p) asm volatile("s_mov_b64 exec, 0xffff\n\tglobal_store_dwordx2 %0, %1, %2 sc1\n\ts_mov_b64 exec, -1" :: "v"(lane * 8), "v"(tag), "s"(p) : "memory")
#define POLLA(z_, p_, w_) asm volatile("s_cmp_lg_u32 %3, 7\n\ts_cbranch_scc1 2f\n\tglobal_load_dwordx2 %0, %1, %2 sc1\n2:" \
    : "=v"(pv) : "v"(z_), "s"(p_), "s"(w_) : "memory", "scc")
#define CHECKA(n, z_, p_, w_, sl_) asm volatile("s_mov_b32 %[sp], 0\n\ts_waitcnt vmcnt(%[nn])\n\ts_cmp_lg_u32 %[wv], 7\n\ts_cbranch_scc1 2f\n" \
    "1:\n\tv_cmp_ne_u64 vcc, %[tg], %[v]\n\ts_nop 4\n\ts_cbranch_vccz 2f\n\t" \
    "s_add_u32 %[sp], %[sp], 1\n\ts_cmp_ge_u32 %[sp], %[lim]\n\ts_cbranch_scc1 2f\n\ts_mov_b64 exec, vcc\n\ts_sleep %[sl]\n\t" \
    "global_load_dwordx2 %[v], %[z], %[p] sc1\n\ts_waitcnt vmcnt(0)\n\ts_branch 1b\n2:\n\ts_mov_b64 exec, -1" \
    : [v] "+v"(pv), [sp] "=&s"(st) : [z] "v"(z_), [p] "s"(p_), [tg] "s"(tag), [lim] "i"(SPIN_MAX), [nn] "i"(n), [sl] "i"(sl_), [wv] "s"(w_) : "memory", "scc", "vcc")
#define POLLM(z_, p_, m_, sc_) asm volatile("s_mov_b64 exec, %3\n\tglobal_load_dwordx2 %0, %1, %2" sc_ "\n\ts_mov_b64 exec, -1" : "=v"(pv) : "v"(z_), "s"(p_), "s"(m_) : "memory")
#define CHECKM(n, z_, p_, m_, sl_) asm volatile("s_mov_b32 %[sp], 0\n\ts_mov_b64 exec, %[m]\n\ts_waitcnt vmcnt(%[nn])\n" \
    "1:\n\tv_cmp_ne_u64 vcc, %[tg], %[v]\n\ts_nop 4\n\ts_cbranch_vccz 2f\n\t" \
    "s_add_u32 %[sp], %[sp], 1\n\ts_cmp_ge_u32 %[sp], %[lim]\n\ts_cbranch_scc1 2f\n\ts_mov_b64 exec, vcc\n\ts_sleep %[sl]\n\t" \
    "global_load_dwordx2 %[v], %[z], %[p] sc1\n\ts_waitcnt vmcnt(0)\n\ts_branch 1b\n2:\n\ts_mov_b64 exec, -1" \
    : [v] "+v"(pv), [sp] "=&s"(st) : [z] "v"(z_), [p] "s"(p_), [tg] "s"(tag), [lim] "i"(SPIN_MAX), [nn] "i"(n), [sl] "i"(sl_), [m] "s"(m_) : "memory", "scc", "vcc")
#define HOLD 2
#define P_GATE 0
#define F_SLEEP 2

constexpr int ATM = 6, NEW = 5;
#define YST(v, sb) asm volatile("global_store_dwordx4 %0, %1, %2 sc1\n\ts_nop 1" :: "v"(yv), "v"(v), "s"(sb) : "memory")

extern "C" __global__ __launch_bounds__(512, 2)
void k_conv(const char* __restrict__ x_, const char* __restrict__ wgt_, char* __restrict__ ws_, char* __restrict__ out_ INSTR_PARAM)
{
  __shared__ __attribute__((aligned(16))) char smem[M_LDS];
  INSTR_DECL;
  u32x8 ka;
  unsigned long long cb, tag, pt, t0;
  u32x2 pw;
  unsigned pl, tf, s3, s4;
  const unsigned sbase = (unsigned)(size_t)(LDS3 char*)smem;
  const int b = blockIdx.x;
  {
    unsigned s0, s1, s2, va, vb, vc, vd, ve, vf, vg;
    unsigned long long t2, t3, vz;
    u32x4 vq;
    asm volatile("s_load_dwordx8 %[ka], %[kp], 0\n\tv_readfirstlane_b32 %[tf], %[id]\n\t"
                 "s_add_i32 m0, %[sb], %[co]\n\tv_mov_b32 %[vb], 0\n\tv_mov_b32 %[va], m0\n\tds_write2_b32 %[va], %[vb], %[vb] offset1:1\n\tds_write_b32 %[va], %[vb] offset:8\n\t"
                 "s_cmp_lt_u32 %[b], 32\n\ts_cbranch_scc1 2f\n\t"
                 "s_and_b32 %[s0], %[b], 7\n\ts_mul_i32 %[s0], %[s0], 28\n\ts_lshr_b32 %[s1], %[b], 3\n\ts_add_i32 %[s0], %[s0], %[s1]\n\ts_add_i32 %[s0], %[s0], -4\n\t"
                 "v_and_b32 %[va], 63, %[id]\n\t"
                 "s_and_b32 %[s1], %[s0], -16\n\ts_lshr_b32 %[s2], %[s0], 1\n\ts_and_b32 %[s2], %[s2], 6\n\ts_add_i32 %[s1], %[s1], %[s2]\n\t"
                 "v_cmp_lt_u32 vcc, 31, %[va]\n\tv_lshlrev_b32 %[va], 4, %[va]\n\t"
                 "s_lshr_b32 %[s2], %[tf], 6\n\ts_mul_i32 %[s2], %[s2], 5\n\ts_and_b32 %[s2], %[s2], 9\n\ts_add_i32 %[s1], %[s1], %[s2]\n\t"
                 "v_xor_b32 %[vc], 32, %[va]\n\t"
                 "s_add_i32 %[s2], %[s1], -6\n\ts_addk_i32 %[s1], 218\n\ts_min_u32 %[s1], %[s1], %[s2]\n\t"
                 "s_and_b32 %[s0], %[s0], 3\n\ts_cselect_b32 %[s2], 0, 43008\n\t"
                 "v_cndmask_b32 %[vb], %[va], %[vc], vcc\n\tv_add_u32 %[vd], %[s2], %[va]\n\t"
                 "s_lshl2_add_u32 %[s1], %[s1], %[s0]\n\ts_mulk_i32 %[s1], 10752\n\ts_addk_i32 %[s1], 2560\n\t"
                 "v_cndmask_b32 %[ve], %[vd], %[vc], vcc\n\t"
                 "s_lshl_b32 %[s2], %[b], 3\n\ts_and_b32 %[s2], %[s2], 0xffffffc0\n\ts_addk_i32 %[s2], -256\n\t"
                 "v_lshrrev_b32 %[vf], 3, %[id]\n\tv_add_u32 %[vf], %[s2], %[vf]\n\tv_min_u32 %[vf], 1763, %[vf]\n\t"
                 "v_mul_u32_u24 %[vg], 10923, %[vf]\n\tv_lshrrev_b32 %[vg], 16, %[vg]\n\tv_mul_u32_u24 %[vg], 18, %[vg]\n\t"
                 "s_and_b32 %[s2], %[b], 3\n\ts_mul_i32 %[s2], %[s2], 6\n\tv_add3_u32 %[vf], %[vf], %[vg], %[s2]\n\tv_lshlrev_b32 %[vf], 7, %[vf]\n\t"
                 "s_lshl_b32 %[s3], %[b], 9\n\ts_add_i32 %[s3], %[s3], %[tf]\n\ts_lshl1_add_u32 %[s3], %[s3], %[fo]\n\t"
                 "s_addk_i32 m0, 256\n\ts_mul_i32 %[s0], %[tf], 192\n\t"
                 "s_waitcnt lgkmcnt(0)\n\tglobal_load_lds_dword %[vf], s[14:15]\n\t"
                 "s_add_u32 vcc_lo, s16, 0x3fffff\n\ts_addc_u32 vcc_hi, s17, 0\n\ts_and_b32 vcc_lo, vcc_lo, 0xffc00000\n\ts_mov_b32 %[s2], %[eo]\n\t"
                 "s_load_dwordx2 %[tg], vcc, %[s2]\n\ts_load_dword %[s4], vcc, %[s3] offset:64\n\ts_load_dword %[s3], vcc, %[s3]\n\ts_add_i32 m0, %[s0], %[sb]\n\t"
                 "s_barrier\n\ts_cmpk_ge_u32 %[tf], 0x100\n\ts_cbranch_scc1 1f\n\ts_setprio 3\n\ts_memrealtime %[t0]\n\t"
                 "s_mov_b64 vcc, s[12:13]\n\ts_add_u32 vcc_lo, vcc_lo, %[s1]\n\ts_addc_u32 vcc_hi, vcc_hi, 0\n\t"
                 "global_load_lds_dwordx4 %[vd], vcc offset:-4096\n\tglobal_load_lds_dwordx4 %[ve], vcc offset:-3072\n\t"
                 "global_load_lds_dwordx4 %[vc], vcc offset:-2048\n\tglobal_load_lds_dwordx4 %[va], vcc offset:-1024\n\t"
                 "global_load_lds_dwordx4 %[vb], vcc\n\tglobal_load_lds_dwordx4 %[vc], vcc offset:1024\n\t"
                 "global_load_lds_dwordx4 %[va], vcc offset:2048\n\tglobal_load_lds_dwordx4 %[vb], vcc offset:3072\n\t"
                 "s_addk_i32 m0, 0x1000\n\ts_add_u32 vcc_lo, vcc_lo, 0x1000\n\ts_addc_u32 vcc_hi, vcc_hi, 0\n\t"
                 "global_load_lds_dwordx4 %[vc], vcc\n\tglobal_load_lds_dwordx4 %[va], vcc offset:1024\n\t"
                 "global_load_lds_dwordx4 %[vb], vcc offset:2048\n\tglobal_load_lds_dwordx4 %[vc], vcc offset:3072\n\t"
                 "s_waitcnt lgkmcnt(0)\n\ts_branch 1f\n"
                 "2:\n\ts_xor_b32 %[s0], %[b], 24\n\ts_lshr_b32 %[s1], %[s0], 3\n\ts_or_b32 %[s1], %[s1], %[tf]\n\ts_cbranch_scc1 1f\n\t"
                 "s_getreg_b32 %[s2], hwreg(HW_REG_XCC_ID)\n\tv_lshlrev_b32 %[va], 4, %[id]\n\tv_lshl_add_u32 %[va], %[s0], 7, %[va]\n\tv_add_u32 %[vc], 0x2000, %[va]\n\t"
                 "s_lshl_b32 %[s0], %[s0], 3\n\tv_mov_b32 %[vb], %[s0]\n\ts_sub_i32 %[s3], %[s2], %[b]\n\ts_and_b32 %[s3], %[s3], 7\n\ts_lshl_b32 %[s3], %[s3], 12\n\t"
                 "s_mov_b64 exec, 0xff\n\ts_waitcnt lgkmcnt(0)\n\t"
                 "s_add_u32 vcc_lo, s16, 0x3fffff\n\ts_addc_u32 vcc_hi, s17, 0\n\ts_and_b32 vcc_lo, vcc_lo, 0xffc00000\n\t"
                 "s_add_u32 vcc_lo, vcc_lo, %[eo]\n\ts_addc_u32 vcc_hi, vcc_hi, 0\n\t"
                 "global_store_dwordx4 %[va], %[vq], vcc offset:256 sc1\n\tglobal_store_dwordx4 %[vc], %[vq], vcc offset:256 sc1\n\t"
                 "s_load_dwordx2 s[26:27], vcc, 0\n\ts_load_dwordx2 s[24:25], vcc, %[s0] offset:128\n\t"
                 "s_waitcnt vmcnt(1)\n\ts_memtime s[20:21]\n\ts_waitcnt vmcnt(0)\n\ts_memtime s[22:23]\n\ts_waitcnt lgkmcnt(0)\n\t"
                 "s_sub_u32 s22, s22, s20\n\ts_min_u32 s22, s22, 255\n\ts_sub_i32 %[s1], 320, s22\n\ts_addk_i32 s22, 192\n\t"
                 "s_bitcmp1_b32 %[s2], 2\n\ts_cselect_b32 s22, %[s1], s22\n\ts_or_b32 s22, s22, %[s3]\n\t"
                 "s_add_u32 s20, s26, %[el]\n\ts_addc_u32 s21, s27, %[eh]\n\ts_and_b32 s20, s20, 0xffff0000\n\ts_or_b32 s20, s20, s22\n\t"
                 "v_mov_b64 %[vz], s[20:21]\n\ts_xor_b64 s[24:25], s[24:25], s[20:21]\n\ts_lshr_b64 s[24:25], s[24:25], 16\n\ts_cselect_b64 exec, 1, 0\n\t"
                 "global_store_dwordx2 %[vb], %[vz], vcc offset:128 sc1\n\ts_mov_b64 exec, -1\n1:"
                 : [ka] "=&{s[12:19]}"(ka), [tf] "=&s"(tf), [t0] "=&{s[20:21]}"(t0), "=&{s[22:23]}"(t2), "=&{s[24:25]}"(t3), [s0] "=&s"(s0), [s1] "=&s"(s1), [s2] "=&s"(s2), [s3] "=&s"(s3), [s4] "=&s"(s4),
                   [va] "=&v"(va), [vb] "=&v"(vb), [vc] "=&v"(vc), [vd] "=&v"(vd), [ve] "=&v"(ve), [vf] "=&v"(vf), [vg] "=&v"(vg), [vq] "=&v"(vq), [vz] "=&v"(vz), [tg] "=&{s[26:27]}"(tag)
                 : [kp] "s"(__builtin_amdgcn_kernarg_segment_ptr()), [id] "v"(threadIdx.x), [b] "s"(b), [sb] "s"(sbase + 4096u), [co] "i"(F_CNT - 4096), [eo] "i"((unsigned)EP_OFF), [el] "i"((unsigned)EP_STEP), [eh] "i"((unsigned)(EP_STEP >> 32)), [fo] "i"((unsigned)CN_OFF - 32768u)
                 : "memory", "scc", "vcc");
  }
  int lane = threadIdx.x & 63;
  const int wave = tf >> 6, w4 = wave & 3;
  int g = lane >> 4, q = (lane >> 2) & 3, p = lane & 3, r;
  const int c = lane & 15;
  char* w_;
  unsigned fo = 0;
#define ARRIVE4(n, sh) do { unsigned n_; \
    asm volatile("s_waitcnt vmcnt(%4)\n\ts_mov_b64 exec, 1\n\tds_add_rtn_u32 %0, %1, %2\n\ts_waitcnt lgkmcnt(0)\n\tv_bfe_u32 %0, %0, %3, 8\n\t" \
                 "v_cmp_eq_u32 vcc, 3, %0\n\ts_nop 4\n\ts_mul_i32 exec_lo, vcc_lo, 0xffff\n\tglobal_store_dwordx2 %5, %6, %7 sc1\n\ts_mov_b64 exec, -1" \
                 : "=&v"(n_) : "v"(sbase + F_CNT), "v"(1u << (sh)), "s"(sh), "i"(n), "v"((threadIdx.x & 63) * 8), "v"(tag), "s"(w_ + CN_OFF + fo) : "memory", "vcc"); } while (0)
  if (__builtin_expect(wave < 4 && b >= F_NW0, 0)) {
    STAMP(0);
    u32x4 tw[2];
    {
      const float fa = (float)((16 * (wave & 1) + c) | 1) * (1.f / 64.f), f0 = __builtin_fmaf(fa, (float)(8 * g), (c & 1) ? 0.25f : 0.f);
#pragma unroll
      for (int s = 0; s < 4; ++s) {
        const float v0 = __builtin_amdgcn_cosf(__builtin_fmaf(fa, (float)(2 * s), f0)), v1 = __builtin_amdgcn_cosf(__builtin_fmaf(fa, (float)(2 * s + 1), f0));
        const unsigned h = pack2(v0, v1);
        tw[0][s] = h;
        tw[1][s] = pack2(v0 - __builtin_bit_cast(float, h << 16), v1 - __builtin_bit_cast(float, h & 0xffff0000u));
      }
    }
    char* ws = w_ = WSP(2);
    const char* ep = ws + EP_OFF;
    SB0();
    const int L = (b & 7) * 28 + ((b - F_NW0) >> 3);
    const int tp = L & 3;
    const int tl = wave >> 1, ct = wave & 1;
    unsigned tb[2][2], ts[2];
#pragma unroll
    for (int hh = 0; hh < 2; ++hh) {
      const unsigned px = 28 * tl + 3 + 8 * g + 4 * hh + q, base = sbase + px * 192 + p * 8, gb = (px & 8) << 2;
      tb[0][hh] = base + gb; tb[1][hh] = base - gb;
      const unsigned ps = 28 * tl + 2 + 32 * (q & 1) + (q >> 1);
      ts[hh] = sbase + g * F_RS + ps * 192 + (((4 * ct + hh) * 32) ^ ((ps & 8) << 2)) + p * 8;
    }
        const u32x4 rs = {(unsigned)(size_t)gXh, (unsigned)((size_t)gXh >> 32) & 0xffffu, NFW * XF, 0x00020000u};
    const unsigned vin = (unsigned)(2 * tp * XT + (L >> 2) * 256) + c * 16;
    const unsigned vo = (unsigned)((8 * ct + 2 * g) * XF + tl * XT) + vin;
    const unsigned long long xw = (unsigned long long)(ws + XW_OFF);
    const u32x4 rs2 = {(unsigned)xw, (unsigned)(xw >> 32) & 0xffffu, NFW * XF2, 0x00020000u};
    const unsigned vi2 = vin + tl * XT;
    unsigned vo2 = (unsigned)(8 * ct + 2 * g) * XF2 + ((vi2 >> 13) << 14) + (vi2 & 8191);
    const unsigned vs = (g < 2) ? g * XC + vi2 : OOB, vs2 = (g < 2) ? g * XC2 + ((vi2 >> 13) << 14) + (vi2 & 8191) : OOB;
    u32x4 sel[2];
#pragma unroll
    for (int i = 0; i < 8; ++i) sel[i >> 2][i & 3] = ((g >> 1) == (i >> 2) && c == 4 * ((i >> 1) & 1) + 2 * (g & 1) + (i & 1)) ? 0x3f803f80u : 0u;
    {
      LANDED(0);
      asm volatile("" : "+v"(tw[0]), "+v"(tw[1]));
      tag += EP_STEP;
    }
    STAMP(1);
    PLD();
    u32x2 da[2][8];
    f32x4 acc[2][4];
    u32x4 o;
#define F_RD(it, k_) TRR(da[(it) & 1][k_], tb[(it) & 1][(k_) & 1], (it) * 32 + ((k_) >> 1) * F_RS)
#define F_EPI(it, m) do { const f32x4 (&e_)[4] = acc[(it) & 1]; const int s_ = ((m) >> 2) * 2; \
      if (((m) & 3) == 1) { o[0] = pack2(e_[0][s_], e_[0][s_ + 1]); o[1] = pack2(e_[1][s_], e_[1][s_ + 1]); } \
      if (((m) & 3) == 2) { o[2] = pack2(e_[2][s_], e_[2][s_ + 1]); o[3] = pack2(e_[3][s_], e_[3][s_ + 1]); } \
      if (((m) & 3) == 3) { if ((it) < 2) BST(o, vo, rs, (it) * XC + ((m) >> 2) * XF); else BST(o, vo2, rs2, ((it) - 2) * XC2 + ((m) >> 2) * XF2); } } while (0)
#pragma unroll
    for (int k = 0; k < 8; ++k) F_RD(0, k);
#pragma unroll
    for (int it = 0; it < 6; ++it) {
      if (it == 1) { STAMP(3); GATE(W_GATE, W_SLOPE, W_OFF); STAMP(2); __builtin_amdgcn_s_setprio(XPR); SB0(); }
      if (it == 3) { STAMP(10); PLC(4); vo2 |= ((((PMASK(pl) ^ 0xaau) >> (4 * ct + g)) & 1u) << 13); STAMP(7); SB0(); }
#pragma unroll
      for (int m = 0; m < 8; ++m) {
        u32x2 (&d_)[8] = da[it & 1];
        if ((m & 1) == 0) WAIT2((it < 5 || m < 4) ? 6 : 10 - m, d_[m], d_[m + 1]);
        f32x4& a_ = acc[it & 1][m >> 1];
        a_ = mfma32(tw[(m & 1) ^ 1], cat(d_[m & 6], d_[m | 1]), (m & 1) ? a_ : Z4);
        SB0();
        if (it < 5) F_RD(it + 1, m);
        else if (m < 4) TRR(da[0][m], ts[m & 1], (m >> 1) * 64);
        if (it > 0) F_EPI(it - 1, m);
        SB0();
      }
    }
    WAIT4(0, da[0][0], da[0][1], da[0][2], da[0][3]);
#pragma unroll
    for (int m = 0; m < 4; ++m) acc[0][m] = mfma32(sel[m & 1], cat(da[0][m & 2], da[0][m | 1]), Z4);
    SB0();
#pragma unroll
    for (int m = 0; m < 8; ++m) F_EPI(5, m);
    if (ct == 0) { BST(pack8(acc[0][0], acc[0][1]), vs, rs, 16 * XF); BST(pack8(acc[0][2], acc[0][3]), vs2, rs2, 16 * XF2); }
    else BST(pack8(acc[0][0], acc[0][1]), vs2, rs2, 16 * XF2 + 2 * XC2);
    STAMP(4);
    __builtin_amdgcn_s_setprio(3);
    if (HOLD < 2) BAR();
    VMW(0);
    ARRIVEL(ws + CN_OFF + (L * 4 + wave) * 128);
    if (HOLD == 2) BAR();
  } else {
    __builtin_amdgcn_s_setprio(1);
    unsigned pf[2];
    {
      unsigned cl;
      asm volatile("s_cmp_ge_u32 %4, %5\n\ts_cbranch_scc1 1f\n\t"
                   "s_getpc_b64 %3\nfftconv_code_pc:\n\t"
                   "s_mov_b32 %0, fftconv_code_end-fftconv_code_pc\n\t"
                   "v_lshlrev_b32 %2, 6, %6\n\tv_min_u32 %1, %0, %2\n\tv_min_u32 %2, 0x4fc0, %2\n\t"
                   "global_load_dword %1, %1, %3\n\tglobal_load_dword %2, %2, %7\n1:"
                   : "=&s"(cl), "=&v"(pf[0]), "=&v"(pf[1]), "=&s"(cb)
                   : "s"(b), "i"(F_NPF), "v"(threadIdx.x), "s"(&kTabM) : "memory", "scc");
    }
    const int bj = min(b >> 4, (b >> 5) + 1), cc = (bj * 11) >> 5, wp = bj - 3 * cc, ioc = 4 * cc + (b & 3);
    const int wi = b >= F_NW0 ? b >> 3 : 2 * (b >> 3) + (wave < 4 ? 1 : 0);
    unsigned wv[2];
#pragma unroll
    for (int i = 0; i < 2; ++i) {
      const unsigned hi = lane >> 5, j = ((lane & 31) >> 1) ^ (4 * i + ((2 * w4 + hi) & 3));
      wv[i] = (w4 < 3 ? hi : 0) * (CH * CH * 2) + ((j >> 2) + 8 * (j & 2)) * (CH * 2) + (j & 1) * 32 + (lane & 1) * 16;
    }
    unsigned oW = (unsigned)((32 * (ioc >> 2) + 4 * (ioc & 3)) * (CH * 2) + 64 * wp + 2 * w4 * (CH * CH * 2));
    asm volatile("" : "+v"(wv[0]), "+v"(wv[1]));
    unsigned xcc;
    asm volatile("s_getreg_b32 %0, hwreg(HW_REG_XCC_ID)" : "=s"(xcc));
    unsigned rot = xcc - b;
    asm volatile("" : "+s"(oW));
    KWAIT();
    STAMP(0);
    char* ws = w_ = WSP(2);
    const char* ep = ws + EP_OFF;
    asm volatile("s_cmp_ge_u32 %2, 32\n\ts_cbranch_scc1 1f\n\ts_load_dwordx2 %0, %1, 0\n\ts_barrier\n1:" : "+s"(tag) : "s"(ep), "s"(b) : "memory", "scc");
    if (b < F_NW0) WDMA();
    STAMP(10);
    SB0();
    u32x4 e[2][2], wq[2][4];
    unsigned ta[2][8];
#pragma unroll
    for (int j = 0; j < 8; ++j) {
      ta[0][j] = sbase + F_WOFF + (8 * g + q) * 512 + ((j ^ (4 * (g & 1) + q)) << 5) + p * 8;
      ta[1][j] = ta[0][j] + (g == 3 ? 8192 : 16384);
      asm volatile("" : "+v"(ta[0][j]), "+v"(ta[1][j]));
    }
#define W_UE() do { u = (b - ((b + rot) & 3) + (wi & 3)) & 7; make_e(comp_of(u, 16 * w4 + c), g, e); \
      asm volatile("" : "+v"(e[0][0]), "+v"(e[0][1]), "+v"(e[1][0]), "+v"(e[1][1])); } while (0)
    unsigned vo, vo1;
    int u;
    W_UE();
    SB0();
    STAMP(1);
    if (b >= F_NW0) WDMA();
    LANDED(1);
    asm volatile("" : "+v"(pf[0]), "+v"(pf[1]), "+s"(t0), "+s"(tag));
    tag += EP_STEP;
    PLD();
    STAMP(9);
   for (bool first = true;;) {
    u32x2 d[3][4];
    f32x4 acc[4];
#define W_RD(n, k_) TRR(d[(n) % 3][k_], ta[(k_) >> 1][(2 * (n) + ((n) >> 3)) & 7], (((n) >> 2) & 1) * 256 + ((k_) & 1) * 2048)
#pragma unroll
    for (int k = 0; k < 8; ++k) W_RD(k >> 2, k & 3);
#pragma unroll
    for (int n = 0; n < 16; ++n) {
      u32x2 (&d_)[4] = d[n % 3];
      WAIT4((n < 15) ? 4 : 0, d_[0], d_[1], d_[2], d_[3]);
#pragma unroll
      for (int m = 0; m < 4; ++m) {
        acc[n & 3] = mfma32(e[m & 1][(m >> 1) ^ 1], cat(d_[2 * (m & 1)], d_[2 * (m & 1) + 1]), m ? acc[n & 3] : Z4);
        SB0();
        if (n < 14) W_RD(n + 2, m);
        if (n > 2 && (n & 1)) wq[(n - 3) >> 3][m][((n - 3) >> 1) & 3] = pack2(acc[(n - 3) & 3][m], acc[(n - 2) & 3][m]);
        SB0();
      }
    }
#pragma unroll
    for (int r = 0; r < 4; ++r) wq[1][r][3] = pack2(acc[2][r], acc[3][r]);
    asm volatile("" : "+v"(wq[0][0]), "+v"(wq[0][1]), "+v"(wq[0][2]), "+v"(wq[0][3]), "+v"(wq[1][0]), "+v"(wq[1][1]), "+v"(wq[1][2]), "+v"(wq[1][3]));
    STAMP(3);
    if (!first) break;
    first = false;
    PLC(0);
    if (__builtin_expect(((pl ^ rot) & 3u) == 0, 1)) break;
    rot = pl;
    W_UE();
   }
    {
      const int cp0 = comp_of(u, 16 * w4 + 4 * g), fw = (cp0 * 2341) >> 16;
      vo = (unsigned)(fw * WF + (cp0 - 28 * fw) * (WK / 2) + (ioc & 3) * 256 + c * 16) | ((fw < 16 ? (PMASK(pl) >> u) ^ fw : pl >> 3) & 1u) << 13;
      vo1 = vo ^ 8192u;
    }
    const u32x4 rsw = {(unsigned)(size_t)ws, (unsigned)((size_t)ws >> 32) & 0xffffu, NFW * WF, 0x00020000u};
    unsigned so[4] = {(unsigned)(wp * WP + (ioc >> 2) * 2048)};
    so[1] = so[0] + WK; so[2] = so[0] + WH; so[3] = so[1] + WH;
    unsigned long long ex = ~0ull >> ((w4 + 1) * 8 & 32);
    asm volatile("" : "+s"(so[0]), "+s"(so[1]), "+s"(so[2]), "+s"(so[3]), "+s"(ex));
#define WST(m, v1) asm volatile("s_mov_b64 exec, %0\n\t" \
      "buffer_store_dwordx4 %8, %1, %3, %4 offen sc1\n\tbuffer_store_dwordx4 %9, %1, %3, %4 offen offset:1024 sc1\n\t" \
      "buffer_store_dwordx4 %10, %1, %3, %5 offen sc1\n\tbuffer_store_dwordx4 %11, %1, %3, %5 offen offset:1024 sc1\n\t" \
      "buffer_store_dwordx4 %12, %2, %3, %6 offen sc1\n\tbuffer_store_dwordx4 %13, %2, %3, %6 offen offset:1024 sc1\n\t" \
      "buffer_store_dwordx4 %14, %2, %3, %7 offen sc1\n\tbuffer_store_dwordx4 %15, %2, %3, %7 offen offset:1024 sc1\n\ts_mov_b64 exec, -1\n\ts_nop 0" \
      :: "s"(m), "v"(vo), "v"(v1), "s"(rsw), "s"(so[0]), "s"(so[1]), "s"(so[2]), "s"(so[3]), \
         "v"(wq[0][0]), "v"(wq[0][1]), "v"(wq[0][2]), "v"(wq[0][3]), "v"(wq[1][0]), "v"(wq[1][1]), "v"(wq[1][2]), "v"(wq[1][3]) : "memory")
    asm volatile("" : "+v"(vo), "+v"(vo1));
    STAMP(7);
    GATE(WG_W, 64, 0);
    STAMP(2);
    const int item = u * 36 + 4 * bj + (b & 3);
    if (w4 == 3) {
      WST(0xffffull << 32, vo1);
      if (b >= F_NW0) { VMW(0); ARRIVE(ws + CN_OFF + FL_V + item * 8); }
    }
    if (HOLD) BAR();
    STAMP(13);
    WST(ex, vo);
    STAMP(4);
    if (!HOLD) BAR();
    if (w4 == 3 && b < F_NW0) { VMW(8); ARRIVE(ws + CN_OFF + FL_V + item * 8); }
    fo = FL_W + item * 128;
    ARRIVE4(0, 16 + 2 * (wave & 4));
  }
  STAMP(5);

  pl = __builtin_amdgcn_readfirstlane(pl);
  unsigned pm = PMASK(pl), cbit, st;
  u32x2 pv;
  char* outp = KPTR(3);
  asm volatile("" : "+s"(pm), "+s"(outp), "+s"(w_), "+s"(tag));
  const int slot = b & 7, lj = b >> 3;
  const int L = b >= F_NW0 ? 28 * slot + lj - 4 : 224 + 8 * (lj ^ 2) + 2 * ((b + pl) & 3) + (((b + pl) >> 2) & 1);
  if (L >= M_NI) {
    if (L == M_NI) {
      asm volatile("s_sleep 127\n\ts_sleep 127" ::: "memory");
#pragma unroll
      for (int i = 0; i < 3; ++i) {
        const unsigned z = min(threadIdx.x + 512u * i, 1183u) * 128u;
        POLLA(z, w_ + CN_OFF, 7); CHECKA(0, z, w_ + CN_OFF, 7, 8);
      }
      BAR();
      if (wave == 0) ARRIVE(w_ + EP_OFF);
    }
    STAMP_MID(); STAMP_END(10);
    return;
  }
  const int fw = L / 14, G = (L - 14 * fw) >> 1, half = L & 1;
  unsigned vL = lane * 16;
  const char* tabp = (const char*)&kTabM;
  u32x4 fh[2][2], fi[2], gi[2][2];
  const int Q = 8 * G + wave;
  unsigned oX = (unsigned)(fw * XF + Q * 2048);
  int k0, k1, mw;
  unsigned xl0;
  const char *xs = gXh + oX, *xsh, *xs2, *xsh2, *ws[2];
  unsigned zw_;
  {
    const int Qz = Q + ((lane >> 3) & 4), t = (Qz * 37) >> 8;
    int i = 8 * (Qz - 7 * t) + min((lane & 31) >> 1, 11);
    i -= i >= 56 ? 56 : 0;
    const unsigned z = (unsigned)((i * 4 + (t >> 1)) * 4 + 2 * (t & 1) + (fw < 16 ? fw >> 3 : lane & 1)) * 128;
    zw_ = fw < 16 ? FL_W + (slot * 36 + min(wave * 5 + min(lane, 4), 35)) * 128 : FL_V + min(wave * 32 + (lane & 31), 251) * 8;
    STAMP(14);
    if (wave < 4) {
      POLLA(z, w_ + CN_OFF, 7); CHECKA(0, z, w_ + CN_OFF, 7, F_SLEEP);
      asm volatile("ds_write_b8 %0, %1 offset:4" :: "v"(sbase + F_CNT + wave), "v"(1u) : "memory");
    } else {
      unsigned n_;
      asm volatile("1:\n\tds_read_u8 %0, %1\n\ts_waitcnt lgkmcnt(0)\n\tv_cmp_eq_u32 vcc, 0, %0\n\ts_nop 4\n\ts_cbranch_vccz 2f\n\ts_sleep 1\n\ts_branch 1b\n2:"
                   : "=&v"(n_) : "v"(sbase + F_CNT + wave) : "memory", "vcc");
    }
    STAMP(15);
  }
  STAMP_MID();
  STAMP(0);
#define POLLW() POLLA(zw_, w_ + CN_OFF, 7)
#define CHECKW(n) CHECKA(n, zw_, w_ + CN_OFF, 7, F_SLEEP)
#define XPIN() asm volatile("" : "+s"(xs))
  char *ys, *Yh;
  char *cnt, *flp;
  u32x4 xr[3][2][2];
  u32x2 xh[3][2];
#define TABF() do { int l_ = L; asm volatile("" : "+s"(l_)); const char* tf = tabp + (l_ >= 224 ? 4096 : 0); xsh = xs + (Q - 7 * ((Q * 37) >> 8) == 6 ? 2048 - XT : 2048); \
    GLD(fh[0][0], vL, tf, 0); GLD(fh[1][0], vL, tf, 2048); if (H_LO) { GLD(fh[0][1], vL, tf, 1024); GLD(fh[1][1], vL, tf, 3072); } } while (0)
#define SETUP_X() do { \
    const int qh_ = Q - 7 * ((Q * 37) >> 8) == 6 ? Q - 6 : Q + 1; \
    unsigned o_ = (unsigned)XW_OFF + fw * XF2 + ((fw >> 1) & 1) * 8192; \
    unsigned o1_ = o_ + (Q >> 2) * 16384 + (Q & 3) * 2048, o2_ = o_ + (qh_ >> 2) * 16384 + (qh_ & 3) * 2048; \
    asm volatile("" : "+s"(o1_), "+s"(o2_)); \
    cbit = ((pm >> slot) & 1u) << 13; \
    xs2 = w_ + (o1_ ^ (fw < 16 ? cbit : 0u)); xsh2 = w_ + (o2_ ^ (fw < 16 ? cbit : 0u)); \
    asm volatile("" : "+s"(w_), "+s"(xs2), "+s"(xsh2)); \
  } while (0)
#define SETUP_P() do { \
    int w8_ = wave; asm volatile("" : "+s"(w8_)); \
    k0 = w8_ < 4 ? 2 * w8_ : 4 + w8_; k1 = w8_ < 4 ? k0 + 1 : 10 + (w8_ >> 1); mw = w8_ < 4 ? 0 : w8_ & 1; \
    xl0 = sbase + w8_ * (4 * M_XT); \
    ws[0] = w_ + (WT_OFF + (((fw * 2 + half) * WH + k0 * WK + (fw & 1) * 8192 + 2048) ^ cbit)); ws[1] = ws[0] + (k1 - k0) * WK; \
    Yh = w_ + YH_OFF; cnt = w_ + CN_OFF + FL_Y; \
    ys = Yh + (Q * YQ + (((pm >> ((Q * 37) >> 8)) ^ (Q >> 1)) & 1) * 8192 + (fw >> 3) * 16384 + (fw & 7) * 1024 + half * 512); \
    flp = cnt + (G * 8 + wave) * CN_G + (fw * 2 + half) * 8;         \
    asm volatile("" : "+v"(vL)); \
    lane = vL >> 4; g = lane >> 4; r = lane & 15; q = (lane >> 2) & 3; p = lane & 3; \
  } while (0)

  unsigned zr[2], yv, to[4];
  u32x10 dv[2];
  f32x4 tacc[4][2][2];
#define SETUP_Z() do { \
    zr[0] = sbase + M_RB + wave * (4 * M_ZT) + (2 * g + (q >> 1)) * 64 + p * 16 + (q & 1) * 8; zr[1] = zr[0] + (g == 3 ? 384 : 512); \
    yv = (g & 1) * YT + (((r >> 1) ^ (fw & 3)) << 6) + (r & 1) * 32 + (g >> 1) * 16; \
  } while (0)
#define TDV(cc) do { _Pragma("unroll") for (int c_ = 0; c_ < 2; ++c_) { const u32x4 a_ = xr[cc][c_][0], b_ = xr[cc][c_][1]; \
      dv[c_] = u32x10{a_[0], a_[1], a_[2], a_[3], b_[0], b_[1], b_[2], b_[3], xh[cc][c_][0], xh[cc][c_][1]}; } \
    asm volatile("" : "+v"(dv[0]), "+v"(dv[1])); } while (0)
#define TMF(htl, chg, ct) do { const u32x4 b_ = __builtin_shufflevector(dv[chg], dv[chg], 2 * (htl), 2 * (htl) + 1, 2 * (htl) + 2, 2 * (htl) + 3); \
    MM(tacc[htl][ct][chg], fh[ct][0], b_, H_LO ? mfma32(fh[ct][1], b_, Z4) : Z4); } while (0)
#define TCV(i, htl, e) do { to[i] = pack2(tacc[htl][(e) >> 2][0][(e) & 3], tacc[htl][(e) >> 2][1][(e) & 3]); \
    if ((e) == 6) asm volatile("" :: "v"(to[i]), "v"(tacc[htl][1][0]), "v"(tacc[htl][1][1])); } while (0)
#define TM0(cc) asm volatile("s_mov_b32 m0, %0\n\ts_nop 0" :: "s"(xl0 + ((cc) == 1 ? 8192 : 0)) : "memory")
#define TWR(i, cc, htl, e) asm volatile("ds_write_addtid_b32 %0 offset:%1" :: "v"(to[i]), "i"(((cc) == 1 ? M_RB - 8192 : 0) + (htl) * M_XT + (e) * 256) : "memory")
#define TABD() DMA(sbase + M_TAB + wave * 1024, vL, tabp + 8192 + wave * 1024, 0)
#define TABI() do { const unsigned a_ = sbase + M_TAB + (fw == 16 ? 2048 : 0) + vL; DSR(fi[0], a_, 0); if (H_LO) DSR(fi[1], a_, 1024); } while (0)
#define TABG() do { const unsigned a_ = sbase + M_TAB + vL; DSR(gi[0][0], a_, 4096); DSR(gi[0][1], a_, 5120); DSR(gi[1][0], a_, 6144); DSR(gi[1][1], a_, 7168); } while (0)
  u32x2 zd[2][4];
#define ZRD(s, zo) do { TRR(zd[s][0], zr[0], zo); TRR(zd[s][1], zr[1], zo); TRR(zd[s][2], zr[0], (zo) + M_ZT); TRR(zd[s][3], zr[1], (zo) + M_ZT); } while (0)
  f32x4 zy[2][2];
  u32x4 zo;
#define ZIW(s, n) WAIT4(n, zd[s][0], zd[s][1], zd[s][2], zd[s][3])
#define ZIM(s, t) do { const u32x4 a_ = cat(zd[s][2 * (t)], zd[s][2 * (t) + 1]); MM(zy[s][t], a_, fi[0], H_LO ? mfma32(a_, fi[1], Z4) : Z4); } while (0)
  unsigned zc[4];
#define ZIC(s, i) do { if ((i) < 2) { zc[2 * (i)] = pack2(zy[s][0][2 * (i)], zy[s][0][2 * (i) + 1]); zc[2 * (i) + 1] = pack2(zy[s][1][2 * (i)], zy[s][1][2 * (i) + 1]); \
      PIN(zc[2 * (i)]); PIN(zc[2 * (i) + 1]); } \
    else { const auto s0_ = __builtin_amdgcn_permlane16_swap(zc[0], zc[1], false, false), s1_ = __builtin_amdgcn_permlane16_swap(zc[2], zc[3], false, false); \
      zo = u32x4{s0_[0], s1_[0], s0_[1], s1_[1]}; PIN(zo); } } while (0)
#define ZINV2(p_) do { ZIW(0, 4); ZIM(0, 0); ZIM(0, 1); ZIW(1, 0); ZIM(1, 0); ZIM(1, 1); SB0(); \
    ZIC(0, 0); ZIC(0, 1); ZIC(0, 2); YST(zo, ys + (p_) * YG); ZIC(1, 0); ZIC(1, 1); ZIC(1, 2); YST(zo, ys + (p_) * YG + 2 * YT); } while (0)
#define flc (cnt + (min(L, I_NWG - 1) >> 2) * CN_G)
#define src (Yh + (((L >> 2) * YQ + (L & 3) * YT + ((L >> 3) & 1) * 8192) ^ cbit))
#define ARRA(p) asm volatile("s_mov_b64 exec, 1\n\tglobal_atomic_swap_x2 %0, %1, %2 sc1\n\ts_mov_b64 exec, -1" :: "v"(0u), "v"(tag), "s"(p) : "memory")
#define ARRY(s_) do { if (ATM >> (s_) & 1) ARRA(flp + (s_) * CN_P); else ARRIVE(flp + (s_) * CN_P); } while (0)
#define POLLT(z_, p_, w_) asm volatile("s_cmp_lg_u32 %3, 7\n\ts_cbranch_scc1 2f\n\ts_mov_b32 exec_hi, 3\n\tglobal_atomic_or_x2 %0, %1, %4, %2 sc0 sc1\n\ts_mov_b32 exec_hi, -1\n2:" \
    : "=&v"(pv) : "v"(z_), "s"(p_), "s"(w_), "v"(0ull) : "memory", "scc")
#define CHECKT(n, z_, p_, w_, sl_) asm volatile("s_mov_b32 %[sp], 0\n\ts_waitcnt vmcnt(%[nn])\n\ts_cmp_lg_u32 %[wv], 7\n\ts_cbranch_scc1 2f\n\ts_mov_b32 exec_hi, 3\n" \
    "1:\n\tv_cmp_ne_u64 vcc, %[tg], %[v]\n\ts_nop 4\n\ts_cbranch_vccz 2f\n\t" \
    "s_add_u32 %[sp], %[sp], 1\n\ts_cmp_ge_u32 %[sp], %[lim]\n\ts_cbranch_scc1 2f\n\ts_mov_b64 exec, vcc\n\ts_sleep %[sl]\n\t" \
    "global_atomic_or_x2 %[v], %[z], %[zero], %[p] sc0 sc1\n\ts_waitcnt vmcnt(0)\n\ts_branch 1b\n2:\n\ts_mov_b64 exec, -1" \
    : [v] "+v"(pv), [sp] "=&s"(st) : [z] "v"(z_), [p] "s"(p_), [tg] "s"(tag), [lim] "i"(SPIN_MAX), [nn] "i"(n), [sl] "i"(sl_), [wv] "s"(w_), [zero] "v"(0ull) : "memory", "scc", "vcc")
#define POLL(s_) POLLT(min(vL >> 1, 264u), flc + (s_) * CN_P, wave)
#define CHECK(n, s_) CHECKT(n, min(vL >> 1, 264u), flc + (s_) * CN_P, wave, POLL_SLEEP)

#define srw (src + wave * 1024)
#define il (sbase + M_RA + wave * 1024)
#define IDMA(s_) do { asm volatile("s_cmp_lg_u32 %3, 0\n\ts_cbranch_scc1 1f\n\ts_mov_b32 m0, %0\n\ts_nop 0\n\tglobal_load_lds_dwordx4 %1, %2 sc1\n1:" \
      :: "s"(il + (s_) * YS + 16384), "v"(vL), "s"(srw + (s_) * YG + 32768), "s"(wave) : "memory", "scc"); \
    DMA1(il + (s_) * YS, vL, srw + (s_) * YG, 0); DMA1(il + (s_) * YS + 8192, vL, srw + (s_) * YG + 16384, 0); } while (0)
  {
    u32x4 wf[36], xt[3][2][2][2];
    f32x4 aP[3][2][2], aQ[3][2][2], aI[3][2][2];
    unsigned xa[2], xb[2], za[2];
#define SETUP_M() do { \
      xa[0] = sbase + r * M_XT + g * 16 + k0 * 128; xa[1] = xa[0] + (k1 - k0) * 128 + mw * (16 * M_XT); xb[0] = xa[0] + M_RB; xb[1] = xa[1] + M_RB; \
      za[0] = sbase + M_RB + r * M_ZT + g * 16 + k0 * 64; za[1] = za[0] + (k1 - k0) * 64 + mw * (16 * M_ZT); \
    } while (0)
#define XRD(cc, kk, ri, mt) DSR(xt[cc][kk][ri][mt], ((cc) == 1 ? xb : xa)[kk], (ri) * 64 + (mt) * (16 * M_XT))
    u32x4 zw;
    float zs[4];
#define ZC(p_, kk, mt, i) do { if ((i) < 2) { zs[2 * (i)] = aP[p_][kk][mt][2 * (i)] - aQ[p_][kk][mt][2 * (i)]; asm volatile("" : "+v"(zs[2 * (i)])); \
      zs[2 * (i) + 1] = aP[p_][kk][mt][2 * (i) + 1] - aQ[p_][kk][mt][2 * (i) + 1]; PIN(zs[2 * (i) + 1]); } \
    else if ((i) == 2) { zw[2] = pack2(aI[p_][kk][mt][0], aI[p_][kk][mt][1]); zw[3] = pack2(aI[p_][kk][mt][2], aI[p_][kk][mt][3]); PIN(zw[2]); PIN(zw[3]); } \
    else { zw[0] = pack2(zs[0], zs[1]); zw[1] = pack2(zs[2], zs[3]); } } while (0)
#define ZW(p_, kk, mt) DSW(za[kk] + ((p_) == 2 ? M_Z1 : 0), zw, ((p_) ? M_Z1 : 0) + (mt) * (16 * M_ZT))
    if (wave < 4) {
    XPIN();
    __builtin_amdgcn_s_setprio(3);
    XLD(0, 0, 0); SB0();
    XLD(0, 1, 0); SB0();
    TABF();
    XLD(0, 0, 1); SB0();
    XLD(0, 1, 1); SB0();
    XLD(0, 0, 2); SB0();
    XLD(0, 1, 2); SB0();
    __builtin_amdgcn_s_setprio(2);
    SETUP_X(); SB0();
    XLD(1, 0, 0); SB0();
    XLD(1, 1, 0); SB0();
    XLD(1, 0, 1); SB0();
    XLD(1, 1, 1); SB0();
    XLD(1, 0, 2); SB0();
    XLD(1, 1, 2); SB0();
    __builtin_amdgcn_s_setprio(0);
    SB0(); SETUP_P(); SETUP_M(); SETUP_Z(); SB0();
    VMW(6); SB0();
    asm volatile("" : XC_(fh[0][0]), XC_(fh[1][0])); SB0();
    STAMP(1);
    TDV(0); SB0();
    TMF(0, 0, 0); SB0();
    TMF(0, 1, 0); SB0();
    XLD(2, 0, 0); SB0();
    TMF(0, 0, 1); SB0();
    TMF(0, 1, 1); SB0();
    XLD(2, 1, 0); SB0();
    TMF(1, 0, 0); SB0();
    TCV(0, 0, 0); SB0();
    TCV(1, 0, 1); SB0();
    TM0(0);
    TWR(0, 0, 0, 0); SB0();
    TCV(2, 0, 2); SB0();
    TMF(1, 1, 0); SB0();
    XLD(2, 0, 1); SB0();
    TWR(1, 0, 0, 1); SB0();
    TCV(3, 0, 3); SB0();
    TWR(2, 0, 0, 2); SB0();
    TMF(1, 0, 1); SB0();
    TCV(0, 0, 4); SB0();
    TWR(3, 0, 0, 3); SB0();
    TCV(1, 0, 5); SB0();
    TWR(0, 0, 0, 4); SB0();
    TMF(1, 1, 1); SB0();
    XLD(2, 1, 1); SB0();
    TCV(2, 0, 6); SB0();
    TWR(1, 0, 0, 5); SB0();
    TWR(2, 0, 0, 6); SB0();
    TMF(2, 0, 0); SB0();
    TCV(3, 1, 0); SB0();
    TCV(0, 1, 1); SB0();
    TWR(3, 0, 1, 0); SB0();
    TCV(1, 1, 2); SB0();
    TMF(2, 1, 0); SB0();
    XLD(2, 0, 2); SB0();
    TWR(0, 0, 1, 1); SB0();
    TCV(2, 1, 3); SB0();
    TWR(1, 0, 1, 2); SB0();
    TMF(2, 0, 1); SB0();
    TCV(3, 1, 4); SB0();
    TWR(2, 0, 1, 3); SB0();
    TCV(0, 1, 5); SB0();
    TWR(3, 0, 1, 4); SB0();
    TMF(2, 1, 1); SB0();
    XLD(2, 1, 2); SB0();
    TCV(1, 1, 6); SB0();
    TWR(0, 0, 1, 5); SB0();
    TWR(1, 0, 1, 6); SB0();
    POLLW(); SB0();
    TMF(3, 0, 0); SB0();
    TCV(2, 2, 0); SB0();
    TCV(3, 2, 1); SB0();
    TWR(2, 0, 2, 0); SB0();
    TCV(0, 2, 2); SB0();
    TMF(3, 1, 0); SB0();
    TWR(3, 0, 2, 1); SB0();
    TCV(1, 2, 3); SB0();
    TWR(0, 0, 2, 2); SB0();
    TMF(3, 0, 1); SB0();
    TABD(); SB0();
    TCV(2, 2, 4); SB0();
    TM0(0);
    TWR(1, 0, 2, 3); SB0();
    TCV(3, 2, 5); SB0();
    TMF(3, 1, 1); SB0();
    TWR(2, 0, 2, 4); SB0();
    TCV(0, 2, 6); SB0();
    TWR(3, 0, 2, 5); SB0();
    TWR(0, 0, 2, 6); SB0();
    VMW(8); SB0();
    TDV(1); SB0();
    TMF(0, 0, 0); SB0();
    TCV(1, 3, 0); SB0();
    TCV(2, 3, 1); SB0();
    TWR(1, 0, 3, 0); SB0();
    TCV(3, 3, 2); SB0();
    TMF(0, 1, 0); SB0();
    TWR(2, 0, 3, 1); SB0();
    TCV(0, 3, 3); SB0();
    TWR(3, 0, 3, 2); SB0();
    TMF(0, 0, 1); SB0();
    TCV(1, 3, 4); SB0();
    TWR(0, 0, 3, 3); SB0();
    TCV(2, 3, 5); SB0();
    TWR(1, 0, 3, 4); SB0();
    TMF(0, 1, 1); SB0();
    TCV(3, 3, 6); SB0();
    TWR(2, 0, 3, 5); SB0();
    TWR(3, 0, 3, 6); SB0();
    TMF(1, 0, 0); SB0();
    TCV(0, 0, 0); SB0();
    TCV(1, 0, 1); SB0();
    TM0(1);
    TWR(0, 1, 0, 0); SB0();
    TCV(2, 0, 2); SB0();
    TMF(1, 1, 0); SB0();
    TWR(1, 1, 0, 1); SB0();
    TCV(3, 0, 3); SB0();
    TWR(2, 1, 0, 2); SB0();
    TMF(1, 0, 1); SB0();
    TCV(0, 0, 4); SB0();
    TWR(3, 1, 0, 3); SB0();
    TCV(1, 0, 5); SB0();
    TWR(0, 1, 0, 4); SB0();
    TMF(1, 1, 1); SB0();
    TCV(2, 0, 6); SB0();
    TWR(1, 1, 0, 5); SB0();
    TWR(2, 1, 0, 6); SB0();
    CHECKW(1); SB0();
    asm volatile("s_waitcnt lgkmcnt(7)" ::: "memory");
    BAR(); SB0();
    STAMP(2);
    WLD(wf[0], vL, ws[0] + 0 * WP, -2048); SB0();
    WLD(wf[1], vL, ws[0] + 0 * WP, -1024); SB0();
    WLD(wf[2], vL, ws[1] + 0 * WP, -2048); SB0();
    WLD(wf[3], vL, ws[1] + 0 * WP, -1024); SB0();
    XRD(0, 0, 0, 0);
    XRD(0, 0, 1, 0);
    XRD(0, 0, 0, 1);
    XRD(0, 0, 1, 1);
    XRD(0, 1, 0, 0);
    XRD(0, 1, 1, 0);
    XRD(0, 1, 0, 1);
    XRD(0, 1, 1, 1);
    SB0();
    TMF(2, 0, 0); SB0();
    WLD(wf[4], vL, ws[0] + 0 * WP, 0); SB0();
    TCV(3, 1, 0); SB0();
    TCV(0, 1, 1); SB0();
    TWR(3, 1, 1, 0); SB0();
    TMF(2, 1, 0); SB0();
    TCV(1, 1, 2); SB0();
    TWR(0, 1, 1, 1); SB0();
    TCV(2, 1, 3); SB0();
    TWR(1, 1, 1, 2); SB0();
    TMF(2, 0, 1); SB0();
    TCV(3, 1, 4); SB0();
    TWR(2, 1, 1, 3); SB0();
    TCV(0, 1, 5); SB0();
    TWR(3, 1, 1, 4); SB0();
    TMF(2, 1, 1); SB0();
    WLD(wf[5], vL, ws[0] + 0 * WP, 1024); SB0();
    TCV(1, 1, 6); SB0();
    TWR(0, 1, 1, 5); SB0();
    TWR(1, 1, 1, 6); SB0();
    TMF(3, 0, 0); SB0();
    TCV(2, 2, 0); SB0();
    TCV(3, 2, 1); SB0();
    TWR(2, 1, 2, 0); SB0();
    TCV(0, 2, 2); SB0();
    TMF(3, 1, 0); SB0();
    TWR(3, 1, 2, 1); SB0();
    TCV(1, 2, 3); SB0();
    TWR(0, 1, 2, 2); SB0();
    TMF(3, 0, 1); SB0();
    WLD(wf[6], vL, ws[1] + 0 * WP, 0); SB0();
    TCV(2, 2, 4); SB0();
    TWR(1, 1, 2, 3); SB0();
    TCV(3, 2, 5); SB0();
    TMF(3, 1, 1); SB0();
    TWR(2, 1, 2, 4); SB0();
    TCV(0, 2, 6); SB0();
    TWR(3, 1, 2, 5); SB0();
    TWR(0, 1, 2, 6); SB0();
    TCV(1, 3, 0); SB0();
    TCV(2, 3, 1); SB0();
    TWR(1, 1, 3, 0); SB0();
    TCV(3, 3, 2); SB0();
    TWR(2, 1, 3, 1); SB0();
    TCV(0, 3, 3); SB0();
    TWR(3, 1, 3, 2); SB0();
    TCV(1, 3, 4); SB0();
    TWR(0, 1, 3, 3); SB0();
    TCV(2, 3, 5); SB0();
    TWR(1, 1, 3, 4); SB0();
    TCV(3, 3, 6); SB0();
    TWR(2, 1, 3, 5); SB0();
    TWR(3, 1, 3, 6); SB0();
    asm volatile("s_waitcnt lgkmcnt(0)" ::: "memory");
    BAR(); SB0();
    STAMP(3);
    XRD(1, 0, 0, 0);
    XRD(1, 0, 1, 0);
    XRD(1, 0, 0, 1);
    XRD(1, 0, 1, 1);
    XRD(1, 1, 0, 0);
    XRD(1, 1, 1, 0);
    XRD(1, 1, 0, 1);
    XRD(1, 1, 1, 1);
    SB0();
    VMW(9); SB0();
    TDV(2); SB0();
    TMF(0, 0, 0); SB0();
    TMF(0, 1, 0); SB0();
    WLD(wf[7], vL, ws[1] + 0 * WP, 1024); SB0();
    TMF(0, 0, 1); SB0();
    TMF(0, 1, 1); SB0();
    WAITV(6, wf[0], wf[1]); SB0();
    asm volatile("" : XC_(xt[0][0][0][0]), XC_(xt[0][0][1][0])); SB0();
    MM(aP[0][0][0], wf[0], xt[0][0][0][0], Z4); SB0();
    WLD(wf[8], vL, ws[0] + 1 * WP, -2048); SB0();
    TCV(0, 0, 0); SB0();
    TCV(1, 0, 1); SB0();
    MM(aQ[0][0][0], wf[1], xt[0][0][1][0], Z4); SB0();
    TM0(2);
    TWR(0, 2, 0, 0); SB0();
    TCV(2, 0, 2); SB0();
    TWR(1, 2, 0, 1); SB0();
    MM(aI[0][0][0], wf[0], xt[0][0][1][0], Z4); SB0();
    TCV(3, 0, 3); SB0();
    TWR(2, 2, 0, 2); SB0();
    TCV(0, 0, 4); SB0();
    MM(aI[0][0][0], wf[1], xt[0][0][0][0], aI[0][0][0]); SB0();
    WLD(wf[9], vL, ws[0] + 1 * WP, -1024); SB0();
    TWR(3, 2, 0, 3); SB0();
    TCV(1, 0, 5); SB0();
    TMF(1, 0, 0); SB0();
    TWR(0, 2, 0, 4); SB0();
    TCV(2, 0, 6); SB0();
    TWR(1, 2, 0, 5); SB0();
    TMF(1, 1, 0); SB0();
    TWR(2, 2, 0, 6); SB0();
    TMF(1, 0, 1); SB0();
    WLD(wf[10], vL, ws[0] + 0 * WP, 2048); SB0();
    TMF(1, 1, 1); SB0();
    asm volatile("" : XC_(xt[0][0][0][1]), XC_(xt[0][0][1][1])); SB0();
    MM(aP[0][0][1], wf[0], xt[0][0][0][1], Z4); SB0();
    TCV(3, 1, 0); SB0();
    TCV(0, 1, 1); SB0();
    TWR(3, 2, 1, 0); SB0();
    MM(aQ[0][0][1], wf[1], xt[0][0][1][1], Z4); SB0();
    WLD(wf[11], vL, ws[0] + 0 * WP, 3072); SB0();
    TCV(1, 1, 2); SB0();
    TWR(0, 2, 1, 1); SB0();
    MM(aI[0][0][1], wf[0], xt[0][0][1][1], Z4); SB0();
    TCV(2, 1, 3); SB0();
    TWR(1, 2, 1, 2); SB0();
    TCV(3, 1, 4); SB0();
    MM(aI[0][0][1], wf[1], xt[0][0][0][1], aI[0][0][1]); SB0();
    TWR(2, 2, 1, 3); SB0();
    TCV(0, 1, 5); SB0();
    TWR(3, 2, 1, 4); SB0();
    TMF(2, 0, 0); SB0();
    WLD(wf[12], vL, ws[1] + 0 * WP, 2048); SB0();
    TCV(1, 1, 6); SB0();
    TWR(0, 2, 1, 5); SB0();
    TMF(2, 1, 0); SB0();
    TWR(1, 2, 1, 6); SB0();
    TMF(2, 0, 1); SB0();
    TMF(2, 1, 1); SB0();
    WLD(wf[13], vL, ws[1] + 0 * WP, 3072); SB0();
    WAITV(10, wf[2], wf[3]); SB0();
    asm volatile("" : XC_(xt[0][1][0][0]), XC_(xt[0][1][1][0])); SB0();
    MM(aP[0][1][0], wf[2], xt[0][1][0][0], Z4); SB0();
    TCV(2, 2, 0); SB0();
    TCV(3, 2, 1); SB0();
    TWR(2, 2, 2, 0); SB0();
    MM(aQ[0][1][0], wf[3], xt[0][1][1][0], Z4); SB0();
    TCV(0, 2, 2); SB0();
    TWR(3, 2, 2, 1); SB0();
    TCV(1, 2, 3); SB0();
    MM(aI[0][1][0], wf[2], xt[0][1][1][0], Z4); SB0();
    WLD(wf[14], vL, ws[0] + 1 * WP, 0); SB0();
    TWR(0, 2, 2, 2); SB0();
    TCV(2, 2, 4); SB0();
    MM(aI[0][1][0], wf[3], xt[0][1][0][0], aI[0][1][0]); SB0();
    TWR(1, 2, 2, 3); SB0();
    TCV(3, 2, 5); SB0();
    TWR(2, 2, 2, 4); SB0();
    TMF(3, 0, 0); SB0();
    TCV(0, 2, 6); SB0();
    TWR(3, 2, 2, 5); SB0();
    TWR(0, 2, 2, 6); SB0();
    TMF(3, 1, 0); SB0();
    WLD(wf[15], vL, ws[0] + 1 * WP, 1024); SB0();
    TMF(3, 0, 1); SB0();
    TMF(3, 1, 1); SB0();
    asm volatile("" : XC_(xt[0][1][0][1]), XC_(xt[0][1][1][1])); SB0();
    MM(aP[0][1][1], wf[2], xt[0][1][0][1], Z4); SB0();
    WLD(wf[16], vL, ws[0] + 1 * WP, 2048); SB0();
    TCV(1, 3, 0); SB0();
    TCV(2, 3, 1); SB0();
    MM(aQ[0][1][1], wf[3], xt[0][1][1][1], Z4); SB0();
    TWR(1, 2, 3, 0); SB0();
    TCV(3, 3, 2); SB0();
    TWR(2, 2, 3, 1); SB0();
    MM(aI[0][1][1], wf[2], xt[0][1][1][1], Z4); SB0();
    TCV(0, 3, 3); SB0();
    TWR(3, 2, 3, 2); SB0();
    TCV(1, 3, 4); SB0();
    MM(aI[0][1][1], wf[3], xt[0][1][0][1], aI[0][1][1]); SB0();
    WLD(wf[17], vL, ws[0] + 1 * WP, 3072); SB0();
    TWR(0, 2, 3, 3); SB0();
    TCV(2, 3, 5); SB0();
    WAITV(12, wf[4], wf[5]); SB0();
    asm volatile("s_waitcnt lgkmcnt(15)" : XC_(xt[1][0][0][0]), XC_(xt[1][0][1][0]) :: "memory"); SB0();
    MM(aP[0][0][0], wf[4], xt[1][0][0][0], aP[0][0][0]); SB0();
    TWR(1, 2, 3, 4); SB0();
    TCV(3, 3, 6); SB0();
    TWR(2, 2, 3, 5); SB0();
    MM(aQ[0][0][0], wf[5], xt[1][0][1][0], aQ[0][0][0]); SB0();
    TWR(3, 2, 3, 6); SB0();
    MM(aI[0][0][0], wf[4], xt[1][0][1][0], aI[0][0][0]); SB0();
    WLD(wf[18], vL, ws[1] + 1 * WP, -2048); SB0();
    MM(aI[0][0][0], wf[5], xt[1][0][0][0], aI[0][0][0]); SB0();
    asm volatile("" : XC_(xt[1][0][0][1]), XC_(xt[1][0][1][1])); SB0();
    MM(aP[0][0][1], wf[4], xt[1][0][0][1], aP[0][0][1]); SB0();
    MM(aQ[0][0][1], wf[5], xt[1][0][1][1], aQ[0][0][1]); SB0();
    WLD(wf[19], vL, ws[1] + 1 * WP, -1024); SB0();
    MM(aI[0][0][1], wf[4], xt[1][0][1][1], aI[0][0][1]); SB0();
    MM(aI[0][0][1], wf[5], xt[1][0][0][1], aI[0][0][1]); SB0();
    asm volatile("s_waitcnt lgkmcnt(0)" ::: "memory");
    BAR(); SB0();
    STAMP(4);
    XRD(2, 0, 0, 0);
    XRD(2, 0, 1, 0);
    XRD(2, 0, 0, 1);
    XRD(2, 0, 1, 1);
    XRD(2, 1, 0, 0);
    XRD(2, 1, 1, 0);
    XRD(2, 1, 0, 1);
    XRD(2, 1, 1, 1);
    SB0();
    WAITV(12, wf[6], wf[7]); SB0();
    asm volatile("" : XC_(xt[1][1][0][0]), XC_(xt[1][1][1][0])); SB0();
    MM(aP[0][1][0], wf[6], xt[1][1][0][0], aP[0][1][0]); SB0();
    WLD(wf[20], vL, ws[1] + 1 * WP, 0); SB0();
    MM(aQ[0][1][0], wf[7], xt[1][1][1][0], aQ[0][1][0]); SB0();
    MM(aI[0][1][0], wf[6], xt[1][1][1][0], aI[0][1][0]); SB0();
    MM(aI[0][1][0], wf[7], xt[1][1][0][0], aI[0][1][0]); SB0();
    WLD(wf[21], vL, ws[1] + 1 * WP, 1024); SB0();
    asm volatile("" : XC_(xt[1][1][0][1]), XC_(xt[1][1][1][1])); SB0();
    MM(aP[0][1][1], wf[6], xt[1][1][0][1], aP[0][1][1]); SB0();
    MM(aQ[0][1][1], wf[7], xt[1][1][1][1], aQ[0][1][1]); SB0();
    MM(aI[0][1][1], wf[6], xt[1][1][1][1], aI[0][1][1]); SB0();
    MM(aI[0][1][1], wf[7], xt[1][1][0][1], aI[0][1][1]); SB0();
    WAITV(12, wf[8], wf[9]); SB0();
    MM(aP[1][0][0], wf[8], xt[0][0][0][0], Z4); SB0();
    WLD(wf[22], vL, ws[1] + 1 * WP, 2048); SB0();
    MM(aQ[1][0][0], wf[9], xt[0][0][1][0], Z4); SB0();
    MM(aI[1][0][0], wf[8], xt[0][0][1][0], Z4); SB0();
    MM(aI[1][0][0], wf[9], xt[0][0][0][0], aI[1][0][0]); SB0();
    WLD(wf[23], vL, ws[1] + 1 * WP, 3072); SB0();
    MM(aP[1][0][1], wf[8], xt[0][0][0][1], Z4); SB0();
    MM(aQ[1][0][1], wf[9], xt[0][0][1][1], Z4); SB0();
    MM(aI[1][0][1], wf[8], xt[0][0][1][1], Z4); SB0();
    MM(aI[1][0][1], wf[9], xt[0][0][0][1], aI[1][0][1]); SB0();
    WAITV(12, wf[10], wf[11]); SB0();
    asm volatile("s_waitcnt lgkmcnt(6)" : XC_(xt[2][0][0][0]), XC_(xt[2][0][1][0]) :: "memory"); SB0();
    MM(aP[0][0][0], wf[10], xt[2][0][0][0], aP[0][0][0]); SB0();
    WLD(wf[24], vL, ws[0] + 2 * WP, -2048); SB0();
    MM(aQ[0][0][0], wf[11], xt[2][0][1][0], aQ[0][0][0]); SB0();
    MM(aI[0][0][0], wf[10], xt[2][0][1][0], aI[0][0][0]); SB0();
    MM(aI[0][0][0], wf[11], xt[2][0][0][0], aI[0][0][0]); SB0();
    WLD(wf[25], vL, ws[0] + 2 * WP, -1024); SB0();
    asm volatile("s_waitcnt lgkmcnt(4)" : XC_(xt[2][0][0][1]), XC_(xt[2][0][1][1]) :: "memory"); SB0();
    MM(aP[0][0][1], wf[10], xt[2][0][0][1], aP[0][0][1]); SB0();
    MM(aQ[0][0][1], wf[11], xt[2][0][1][1], aQ[0][0][1]); SB0();
    ZC(0, 0, 0, 0); SB0();
    MM(aI[0][0][1], wf[10], xt[2][0][1][1], aI[0][0][1]); SB0();
    ZC(0, 0, 0, 1); SB0();
    MM(aI[0][0][1], wf[11], xt[2][0][0][1], aI[0][0][1]); SB0();
    ZC(0, 0, 0, 2); SB0();
    WAITV(12, wf[12], wf[13]); SB0();
    asm volatile("s_waitcnt lgkmcnt(2)" : XC_(xt[2][1][0][0]), XC_(xt[2][1][1][0]) :: "memory"); SB0();
    MM(aP[0][1][0], wf[12], xt[2][1][0][0], aP[0][1][0]); SB0();
    WLD(wf[26], vL, ws[0] + 2 * WP, 0); SB0();
    ZC(0, 0, 0, 3); SB0();
    MM(aQ[0][1][0], wf[13], xt[2][1][1][0], aQ[0][1][0]); SB0();
    ZW(0, 0, 0); SB0();
    ZC(0, 0, 1, 0); SB0();
    MM(aI[0][1][0], wf[12], xt[2][1][1][0], aI[0][1][0]); SB0();
    ZC(0, 0, 1, 1); SB0();
    MM(aI[0][1][0], wf[13], xt[2][1][0][0], aI[0][1][0]); SB0();
    WLD(wf[27], vL, ws[0] + 2 * WP, 1024); SB0();
    ZC(0, 0, 1, 2); SB0();
    asm volatile("s_waitcnt lgkmcnt(1)" : XC_(xt[2][1][0][1]), XC_(xt[2][1][1][1]) :: "memory"); SB0();
    MM(aP[0][1][1], wf[12], xt[2][1][0][1], aP[0][1][1]); SB0();
    ZC(0, 0, 1, 3); SB0();
    ZW(0, 0, 1); SB0();
    MM(aQ[0][1][1], wf[13], xt[2][1][1][1], aQ[0][1][1]); SB0();
    ZC(0, 1, 0, 0); SB0();
    MM(aI[0][1][1], wf[12], xt[2][1][1][1], aI[0][1][1]); SB0();
    ZC(0, 1, 0, 1); SB0();
    MM(aI[0][1][1], wf[13], xt[2][1][0][1], aI[0][1][1]); SB0();
    ZC(0, 1, 0, 2); SB0();
    WAITV(12, wf[14], wf[15]); SB0();
    MM(aP[1][0][0], wf[14], xt[1][0][0][0], aP[1][0][0]); SB0();
    WLD(wf[28], vL, ws[0] + 2 * WP, 2048); SB0();
    ZC(0, 1, 0, 3); SB0();
    MM(aQ[1][0][0], wf[15], xt[1][0][1][0], aQ[1][0][0]); SB0();
    ZW(0, 1, 0); SB0();
    ZC(0, 1, 1, 0); SB0();
    MM(aI[1][0][0], wf[14], xt[1][0][1][0], aI[1][0][0]); SB0();
    ZC(0, 1, 1, 1); SB0();
    MM(aI[1][0][0], wf[15], xt[1][0][0][0], aI[1][0][0]); SB0();
    WLD(wf[29], vL, ws[0] + 2 * WP, 3072); SB0();
    ZC(0, 1, 1, 2); SB0();
    MM(aP[1][0][1], wf[14], xt[1][0][0][1], aP[1][0][1]); SB0();
    ZC(0, 1, 1, 3); SB0();
    ZW(0, 1, 1); SB0();
    MM(aQ[1][0][1], wf[15], xt[1][0][1][1], aQ[1][0][1]); SB0();
    MM(aI[1][0][1], wf[14], xt[1][0][1][1], aI[1][0][1]); SB0();
    MM(aI[1][0][1], wf[15], xt[1][0][0][1], aI[1][0][1]); SB0();
    STAMP(12);
    LGW0(); BAR(); SB0();
    STAMP(13);
    TABI();
    ZRD(0, 0); ZRD(1, 2 * M_ZT); SB0();
    WAITV(12, wf[16], wf[17]); SB0();
    MM(aP[1][0][0], wf[16], xt[2][0][0][0], aP[1][0][0]); SB0();
    WLD(wf[30], vL, ws[1] + 2 * WP, -2048); SB0();
    MM(aQ[1][0][0], wf[17], xt[2][0][1][0], aQ[1][0][0]); SB0();
    MM(aI[1][0][0], wf[16], xt[2][0][1][0], aI[1][0][0]); SB0();
    MM(aI[1][0][0], wf[17], xt[2][0][0][0], aI[1][0][0]); SB0();
    WLD(wf[31], vL, ws[1] + 2 * WP, -1024); SB0();
    ZIW(0, 4); SB0();
    asm volatile("" : XC_(fi[0])); SB0();
    ZIM(0, 0); SB0();
    ZIM(0, 1); SB0();
    ZC(1, 0, 0, 0); SB0();
    ZIW(1, 0); SB0();
    ZIM(1, 0); SB0();
    ZC(1, 0, 0, 1); SB0();
    ZIM(1, 1); SB0();
    ZC(1, 0, 0, 2); SB0();
    MM(aP[1][0][1], wf[16], xt[2][0][0][1], aP[1][0][1]); SB0();
    MM(aQ[1][0][1], wf[17], xt[2][0][1][1], aQ[1][0][1]); SB0();
    ZIC(0, 0); SB0();
    MM(aI[1][0][1], wf[16], xt[2][0][1][1], aI[1][0][1]); SB0();
    ZIC(0, 1); SB0();
    MM(aI[1][0][1], wf[17], xt[2][0][0][1], aI[1][0][1]); SB0();
    ZIC(0, 2); SB0();
    WAITV(12, wf[18], wf[19]); SB0();
    MM(aP[1][1][0], wf[18], xt[0][1][0][0], Z4); SB0();
    WLD(wf[32], vL, ws[1] + 2 * WP, 0); SB0();
    YST(zo, ys + 0 * YT + 0 * YG); SB0();
    MM(aQ[1][1][0], wf[19], xt[0][1][1][0], Z4); SB0();
    ZIC(1, 0); SB0();
    MM(aI[1][1][0], wf[18], xt[0][1][1][0], Z4); SB0();
    ZIC(1, 1); SB0();
    MM(aI[1][1][0], wf[19], xt[0][1][0][0], aI[1][1][0]); SB0();
    WLD(wf[33], vL, ws[1] + 2 * WP, 1024); SB0();
    ZIC(1, 2); SB0();
    MM(aP[1][1][1], wf[18], xt[0][1][0][1], Z4); SB0();
    YST(zo, ys + 2 * YT + 0 * YG); SB0();
    ZC(1, 0, 0, 3); SB0();
    MM(aQ[1][1][1], wf[19], xt[0][1][1][1], Z4); SB0();
    ZW(1, 0, 0); SB0();
    ZC(1, 0, 1, 0); SB0();
    MM(aI[1][1][1], wf[18], xt[0][1][1][1], Z4); SB0();
    ZC(1, 0, 1, 1); SB0();
    MM(aI[1][1][1], wf[19], xt[0][1][0][1], aI[1][1][1]); SB0();
    ZC(1, 0, 1, 2); SB0();
    WAITV(14, wf[20], wf[21]); SB0();
    MM(aP[1][1][0], wf[20], xt[1][1][0][0], aP[1][1][0]); SB0();
    WLD(wf[34], vL, ws[1] + 2 * WP, 2048); SB0();
    ZC(1, 0, 1, 3); SB0();
    MM(aQ[1][1][0], wf[21], xt[1][1][1][0], aQ[1][1][0]); SB0();
    ZW(1, 0, 1); SB0();
    MM(aI[1][1][0], wf[20], xt[1][1][1][0], aI[1][1][0]); SB0();
    MM(aI[1][1][0], wf[21], xt[1][1][0][0], aI[1][1][0]); SB0();
    WLD(wf[35], vL, ws[1] + 2 * WP, 3072); SB0();
    MM(aP[1][1][1], wf[20], xt[1][1][0][1], aP[1][1][1]); SB0();
    MM(aQ[1][1][1], wf[21], xt[1][1][1][1], aQ[1][1][1]); SB0();
    MM(aI[1][1][1], wf[20], xt[1][1][1][1], aI[1][1][1]); SB0();
    MM(aI[1][1][1], wf[21], xt[1][1][0][1], aI[1][1][1]); SB0();
    WAITV(14, wf[22], wf[23]); SB0();
    MM(aP[1][1][0], wf[22], xt[2][1][0][0], aP[1][1][0]); SB0();
    MM(aQ[1][1][0], wf[23], xt[2][1][1][0], aQ[1][1][0]); SB0();
    MM(aI[1][1][0], wf[22], xt[2][1][1][0], aI[1][1][0]); SB0();
    MM(aI[1][1][0], wf[23], xt[2][1][0][0], aI[1][1][0]); SB0();
    MM(aP[1][1][1], wf[22], xt[2][1][0][1], aP[1][1][1]); SB0();
    MM(aQ[1][1][1], wf[23], xt[2][1][1][1], aQ[1][1][1]); SB0();
    ZC(1, 1, 0, 0); SB0();
    MM(aI[1][1][1], wf[22], xt[2][1][1][1], aI[1][1][1]); SB0();
    ZC(1, 1, 0, 1); SB0();
    MM(aI[1][1][1], wf[23], xt[2][1][0][1], aI[1][1][1]); SB0();
    ZC(1, 1, 0, 2); SB0();
    WAITV(12, wf[24], wf[25]); SB0();
    MM(aP[2][0][0], wf[24], xt[0][0][0][0], Z4); SB0();
    ZC(1, 1, 0, 3); SB0();
    ZW(1, 1, 0); SB0();
    MM(aQ[2][0][0], wf[25], xt[0][0][1][0], Z4); SB0();
    ZC(1, 1, 1, 0); SB0();
    MM(aI[2][0][0], wf[24], xt[0][0][1][0], Z4); SB0();
    ZC(1, 1, 1, 1); SB0();
    MM(aI[2][0][0], wf[25], xt[0][0][0][0], aI[2][0][0]); SB0();
    ZC(1, 1, 1, 2); SB0();
    MM(aP[2][0][1], wf[24], xt[0][0][0][1], Z4); SB0();
    ZC(1, 1, 1, 3); SB0();
    ZW(1, 1, 1); SB0();
    MM(aQ[2][0][1], wf[25], xt[0][0][1][1], Z4); SB0();
    MM(aI[2][0][1], wf[24], xt[0][0][1][1], Z4); SB0();
    MM(aI[2][0][1], wf[25], xt[0][0][0][1], aI[2][0][1]); SB0();
    STAMP(14);
    LGW0(); BAR(); SB0();
    STAMP(15);
    ZRD(0, M_Z1 + 0); ZRD(1, M_Z1 + 2 * M_ZT); SB0();
    WAITV(10, wf[26], wf[27]); SB0();
    MM(aP[2][0][0], wf[26], xt[1][0][0][0], aP[2][0][0]); SB0();
    VMW(2); ARRY(0); SB0();
    MM(aQ[2][0][0], wf[27], xt[1][0][1][0], aQ[2][0][0]); SB0();
    MM(aI[2][0][0], wf[26], xt[1][0][1][0], aI[2][0][0]); SB0();
    MM(aI[2][0][0], wf[27], xt[1][0][0][0], aI[2][0][0]); SB0();
    ZIW(0, 4); SB0();
    ZIM(0, 0); SB0();
    ZIM(0, 1); SB0();
    ZIW(1, 0); SB0();
    ZIM(1, 0); SB0();
    ZIM(1, 1); SB0();
    MM(aP[2][0][1], wf[26], xt[1][0][0][1], aP[2][0][1]); SB0();
    MM(aQ[2][0][1], wf[27], xt[1][0][1][1], aQ[2][0][1]); SB0();
    ZIC(0, 0); SB0();
    MM(aI[2][0][1], wf[26], xt[1][0][1][1], aI[2][0][1]); SB0();
    ZIC(0, 1); SB0();
    MM(aI[2][0][1], wf[27], xt[1][0][0][1], aI[2][0][1]); SB0();
    ZIC(0, 2); SB0();
    WAITV(9, wf[28], wf[29]); SB0();
    MM(aP[2][0][0], wf[28], xt[2][0][0][0], aP[2][0][0]); SB0();
    YST(zo, ys + 0 * YT + 1 * YG); SB0();
    ZIC(1, 0); SB0();
    MM(aQ[2][0][0], wf[29], xt[2][0][1][0], aQ[2][0][0]); SB0();
    ZIC(1, 1); SB0();
    MM(aI[2][0][0], wf[28], xt[2][0][1][0], aI[2][0][0]); SB0();
    ZIC(1, 2); SB0();
    MM(aI[2][0][0], wf[29], xt[2][0][0][0], aI[2][0][0]); SB0();
    YST(zo, ys + 2 * YT + 1 * YG); SB0();
    MM(aP[2][0][1], wf[28], xt[2][0][0][1], aP[2][0][1]); SB0();
    MM(aQ[2][0][1], wf[29], xt[2][0][1][1], aQ[2][0][1]); SB0();
    ZC(2, 0, 0, 0); SB0();
    MM(aI[2][0][1], wf[28], xt[2][0][1][1], aI[2][0][1]); SB0();
    ZC(2, 0, 0, 1); SB0();
    MM(aI[2][0][1], wf[29], xt[2][0][0][1], aI[2][0][1]); SB0();
    ZC(2, 0, 0, 2); SB0();
    WAITV(9, wf[30], wf[31]); SB0();
    MM(aP[2][1][0], wf[30], xt[0][1][0][0], Z4); SB0();
    ZC(2, 0, 0, 3); SB0();
    ZW(2, 0, 0); SB0();
    MM(aQ[2][1][0], wf[31], xt[0][1][1][0], Z4); SB0();
    ZC(2, 0, 1, 0); SB0();
    MM(aI[2][1][0], wf[30], xt[0][1][1][0], Z4); SB0();
    ZC(2, 0, 1, 1); SB0();
    MM(aI[2][1][0], wf[31], xt[0][1][0][0], aI[2][1][0]); SB0();
    ZC(2, 0, 1, 2); SB0();
    MM(aP[2][1][1], wf[30], xt[0][1][0][1], Z4); SB0();
    ZC(2, 0, 1, 3); SB0();
    ZW(2, 0, 1); SB0();
    MM(aQ[2][1][1], wf[31], xt[0][1][1][1], Z4); SB0();
    MM(aI[2][1][1], wf[30], xt[0][1][1][1], Z4); SB0();
    MM(aI[2][1][1], wf[31], xt[0][1][0][1], aI[2][1][1]); SB0();
    WAITV(6, wf[32], wf[33]); SB0();
    MM(aP[2][1][0], wf[32], xt[1][1][0][0], aP[2][1][0]); SB0();
    MM(aQ[2][1][0], wf[33], xt[1][1][1][0], aQ[2][1][0]); SB0();
    MM(aI[2][1][0], wf[32], xt[1][1][1][0], aI[2][1][0]); SB0();
    MM(aI[2][1][0], wf[33], xt[1][1][0][0], aI[2][1][0]); SB0();
    MM(aP[2][1][1], wf[32], xt[1][1][0][1], aP[2][1][1]); SB0();
    MM(aQ[2][1][1], wf[33], xt[1][1][1][1], aQ[2][1][1]); SB0();
    MM(aI[2][1][1], wf[32], xt[1][1][1][1], aI[2][1][1]); SB0();
    MM(aI[2][1][1], wf[33], xt[1][1][0][1], aI[2][1][1]); SB0();
    WAITV(3, wf[34], wf[35]); SB0();
    MM(aP[2][1][0], wf[34], xt[2][1][0][0], aP[2][1][0]); SB0();
    MM(aQ[2][1][0], wf[35], xt[2][1][1][0], aQ[2][1][0]); SB0();
    MM(aI[2][1][0], wf[34], xt[2][1][1][0], aI[2][1][0]); SB0();
    MM(aI[2][1][0], wf[35], xt[2][1][0][0], aI[2][1][0]); SB0();
    MM(aP[2][1][1], wf[34], xt[2][1][0][1], aP[2][1][1]); SB0();
    MM(aQ[2][1][1], wf[35], xt[2][1][1][1], aQ[2][1][1]); SB0();
    ZC(2, 1, 0, 0); SB0();
    MM(aI[2][1][1], wf[34], xt[2][1][1][1], aI[2][1][1]); SB0();
    ZC(2, 1, 0, 1); SB0();
    MM(aI[2][1][1], wf[35], xt[2][1][0][1], aI[2][1][1]); SB0();
    ZC(2, 1, 0, 2); SB0();
    STAMP(5);
    ZC(2, 1, 0, 3); SB0();
    ZW(2, 1, 0); SB0();
    ZC(2, 1, 1, 0); SB0();
    ZC(2, 1, 1, 1); SB0();
    ZC(2, 1, 1, 2); SB0();
    ZC(2, 1, 1, 3); SB0();
    ZW(2, 1, 1); SB0();
    VMW(0); ARRY(1); SB0();
    STAMP(1);
    STAMP(2);
    LGW0(); BAR(); SB0();
    } else {
    XPIN();
    __builtin_amdgcn_s_setprio(3);
    XLD(0, 0, 0); SB0();
    XLD(0, 1, 0); SB0();
    TABF();
    XLD(0, 0, 1); SB0();
    XLD(0, 1, 1); SB0();
    XLD(0, 0, 2); SB0();
    XLD(0, 1, 2); SB0();
    __builtin_amdgcn_s_setprio(2);
    SETUP_X(); SB0();
    XLD(1, 0, 0); SB0();
    XLD(1, 1, 0); SB0();
    XLD(1, 0, 1); SB0();
    XLD(1, 1, 1); SB0();
    XLD(1, 0, 2); SB0();
    XLD(1, 1, 2); SB0();
    __builtin_amdgcn_s_setprio(0);
    SB0(); SETUP_P(); SETUP_M(); SETUP_Z(); SB0();
    VMW(6); SB0();
    asm volatile("" : XC_(fh[0][0]), XC_(fh[1][0])); SB0();
    STAMP(1);
    TDV(0); SB0();
    TMF(0, 0, 0); SB0();
    TMF(0, 1, 0); SB0();
    XLD(2, 0, 0); SB0();
    TMF(0, 0, 1); SB0();
    TMF(0, 1, 1); SB0();
    XLD(2, 1, 0); SB0();
    TMF(1, 0, 0); SB0();
    TCV(0, 0, 0); SB0();
    TCV(1, 0, 1); SB0();
    TM0(0);
    TWR(0, 0, 0, 0); SB0();
    TCV(2, 0, 2); SB0();
    TMF(1, 1, 0); SB0();
    XLD(2, 0, 1); SB0();
    TWR(1, 0, 0, 1); SB0();
    TCV(3, 0, 3); SB0();
    TWR(2, 0, 0, 2); SB0();
    TMF(1, 0, 1); SB0();
    TCV(0, 0, 4); SB0();
    TWR(3, 0, 0, 3); SB0();
    TCV(1, 0, 5); SB0();
    TWR(0, 0, 0, 4); SB0();
    TMF(1, 1, 1); SB0();
    XLD(2, 1, 1); SB0();
    TCV(2, 0, 6); SB0();
    TWR(1, 0, 0, 5); SB0();
    TWR(2, 0, 0, 6); SB0();
    TMF(2, 0, 0); SB0();
    TCV(3, 1, 0); SB0();
    TCV(0, 1, 1); SB0();
    TWR(3, 0, 1, 0); SB0();
    TCV(1, 1, 2); SB0();
    TMF(2, 1, 0); SB0();
    XLD(2, 0, 2); SB0();
    TWR(0, 0, 1, 1); SB0();
    TCV(2, 1, 3); SB0();
    TWR(1, 0, 1, 2); SB0();
    TMF(2, 0, 1); SB0();
    TCV(3, 1, 4); SB0();
    TWR(2, 0, 1, 3); SB0();
    TCV(0, 1, 5); SB0();
    TWR(3, 0, 1, 4); SB0();
    TMF(2, 1, 1); SB0();
    XLD(2, 1, 2); SB0();
    TCV(1, 1, 6); SB0();
    TWR(0, 0, 1, 5); SB0();
    TWR(1, 0, 1, 6); SB0();
    POLLW(); SB0();
    TMF(3, 0, 0); SB0();
    TCV(2, 2, 0); SB0();
    TCV(3, 2, 1); SB0();
    TWR(2, 0, 2, 0); SB0();
    TCV(0, 2, 2); SB0();
    TMF(3, 1, 0); SB0();
    TWR(3, 0, 2, 1); SB0();
    TCV(1, 2, 3); SB0();
    TWR(0, 0, 2, 2); SB0();
    TMF(3, 0, 1); SB0();
    TABD(); SB0();
    TCV(2, 2, 4); SB0();
    TM0(0);
    TWR(1, 0, 2, 3); SB0();
    TCV(3, 2, 5); SB0();
    TMF(3, 1, 1); SB0();
    TWR(2, 0, 2, 4); SB0();
    TCV(0, 2, 6); SB0();
    TWR(3, 0, 2, 5); SB0();
    TWR(0, 0, 2, 6); SB0();
    VMW(8); SB0();
    TDV(1); SB0();
    TMF(0, 0, 0); SB0();
    TCV(1, 3, 0); SB0();
    TCV(2, 3, 1); SB0();
    TWR(1, 0, 3, 0); SB0();
    TCV(3, 3, 2); SB0();
    TMF(0, 1, 0); SB0();
    TWR(2, 0, 3, 1); SB0();
    TCV(0, 3, 3); SB0();
    TWR(3, 0, 3, 2); SB0();
    TMF(0, 0, 1); SB0();
    TCV(1, 3, 4); SB0();
    TWR(0, 0, 3, 3); SB0();
    TCV(2, 3, 5); SB0();
    TWR(1, 0, 3, 4); SB0();
    TMF(0, 1, 1); SB0();
    TCV(3, 3, 6); SB0();
    TWR(2, 0, 3, 5); SB0();
    TWR(3, 0, 3, 6); SB0();
    TMF(1, 0, 0); SB0();
    TCV(0, 0, 0); SB0();
    TCV(1, 0, 1); SB0();
    TM0(1);
    TWR(0, 1, 0, 0); SB0();
    TCV(2, 0, 2); SB0();
    TMF(1, 1, 0); SB0();
    TWR(1, 1, 0, 1); SB0();
    TCV(3, 0, 3); SB0();
    TWR(2, 1, 0, 2); SB0();
    TMF(1, 0, 1); SB0();
    TCV(0, 0, 4); SB0();
    TWR(3, 1, 0, 3); SB0();
    TCV(1, 0, 5); SB0();
    TWR(0, 1, 0, 4); SB0();
    TMF(1, 1, 1); SB0();
    TCV(2, 0, 6); SB0();
    TWR(1, 1, 0, 5); SB0();
    TWR(2, 1, 0, 6); SB0();
    CHECKW(1); SB0();
    asm volatile("s_waitcnt lgkmcnt(7)" ::: "memory");
    BAR(); SB0();
    STAMP(2);
    WLD(wf[0], vL, ws[0] + 0 * WP, -2048); SB0();
    WLD(wf[1], vL, ws[0] + 0 * WP, -1024); SB0();
    WLD(wf[2], vL, ws[1] + 0 * WP, -2048); SB0();
    WLD(wf[3], vL, ws[1] + 0 * WP, -1024); SB0();
    XRD(0, 0, 0, 0);
    XRD(0, 0, 1, 0);
    XRD(0, 0, 0, 1);
    XRD(0, 0, 1, 1);
    XRD(0, 1, 0, 0);
    XRD(0, 1, 1, 0);
    SB0();
    TMF(2, 0, 0); SB0();
    WLD(wf[4], vL, ws[0] + 0 * WP, 0); SB0();
    TCV(3, 1, 0); SB0();
    TCV(0, 1, 1); SB0();
    TWR(3, 1, 1, 0); SB0();
    TMF(2, 1, 0); SB0();
    TCV(1, 1, 2); SB0();
    TWR(0, 1, 1, 1); SB0();
    TCV(2, 1, 3); SB0();
    TWR(1, 1, 1, 2); SB0();
    TMF(2, 0, 1); SB0();
    TCV(3, 1, 4); SB0();
    TWR(2, 1, 1, 3); SB0();
    TCV(0, 1, 5); SB0();
    TWR(3, 1, 1, 4); SB0();
    TMF(2, 1, 1); SB0();
    WLD(wf[5], vL, ws[0] + 0 * WP, 1024); SB0();
    TCV(1, 1, 6); SB0();
    TWR(0, 1, 1, 5); SB0();
    TWR(1, 1, 1, 6); SB0();
    TMF(3, 0, 0); SB0();
    TCV(2, 2, 0); SB0();
    TCV(3, 2, 1); SB0();
    TWR(2, 1, 2, 0); SB0();
    TCV(0, 2, 2); SB0();
    TMF(3, 1, 0); SB0();
    TWR(3, 1, 2, 1); SB0();
    TCV(1, 2, 3); SB0();
    TWR(0, 1, 2, 2); SB0();
    TMF(3, 0, 1); SB0();
    WLD(wf[6], vL, ws[1] + 0 * WP, 0); SB0();
    TCV(2, 2, 4); SB0();
    TWR(1, 1, 2, 3); SB0();
    TCV(3, 2, 5); SB0();
    TMF(3, 1, 1); SB0();
    TWR(2, 1, 2, 4); SB0();
    TCV(0, 2, 6); SB0();
    TWR(3, 1, 2, 5); SB0();
    TWR(0, 1, 2, 6); SB0();
    TCV(1, 3, 0); SB0();
    TCV(2, 3, 1); SB0();
    TWR(1, 1, 3, 0); SB0();
    TCV(3, 3, 2); SB0();
    TWR(2, 1, 3, 1); SB0();
    TCV(0, 3, 3); SB0();
    TWR(3, 1, 3, 2); SB0();
    TCV(1, 3, 4); SB0();
    TWR(0, 1, 3, 3); SB0();
    TCV(2, 3, 5); SB0();
    TWR(1, 1, 3, 4); SB0();
    TCV(3, 3, 6); SB0();
    TWR(2, 1, 3, 5); SB0();
    TWR(3, 1, 3, 6); SB0();
    asm volatile("s_waitcnt lgkmcnt(0)" ::: "memory");
    BAR(); SB0();
    STAMP(3);
    XRD(1, 0, 0, 0);
    XRD(1, 0, 1, 0);
    XRD(1, 0, 0, 1);
    XRD(1, 0, 1, 1);
    XRD(1, 1, 0, 0);
    XRD(1, 1, 1, 0);
    SB0();
    VMW(9); SB0();
    TDV(2); SB0();
    TMF(0, 0, 0); SB0();
    TMF(0, 1, 0); SB0();
    WLD(wf[7], vL, ws[1] + 0 * WP, 1024); SB0();
    TMF(0, 0, 1); SB0();
    TMF(0, 1, 1); SB0();
    WAITV(6, wf[0], wf[1]); SB0();
    asm volatile("" : XC_(xt[0][0][0][0]), XC_(xt[0][0][1][0])); SB0();
    MM(aP[0][0][0], wf[0], xt[0][0][0][0], Z4); SB0();
    WLD(wf[8], vL, ws[0] + 1 * WP, -2048); SB0();
    TCV(0, 0, 0); SB0();
    TCV(1, 0, 1); SB0();
    MM(aQ[0][0][0], wf[1], xt[0][0][1][0], Z4); SB0();
    TM0(2);
    TWR(0, 2, 0, 0); SB0();
    TCV(2, 0, 2); SB0();
    TWR(1, 2, 0, 1); SB0();
    MM(aI[0][0][0], wf[0], xt[0][0][1][0], Z4); SB0();
    TCV(3, 0, 3); SB0();
    TWR(2, 2, 0, 2); SB0();
    TCV(0, 0, 4); SB0();
    MM(aI[0][0][0], wf[1], xt[0][0][0][0], aI[0][0][0]); SB0();
    WLD(wf[9], vL, ws[0] + 1 * WP, -1024); SB0();
    TWR(3, 2, 0, 3); SB0();
    TCV(1, 0, 5); SB0();
    TMF(1, 0, 0); SB0();
    TWR(0, 2, 0, 4); SB0();
    TCV(2, 0, 6); SB0();
    TWR(1, 2, 0, 5); SB0();
    TMF(1, 1, 0); SB0();
    TWR(2, 2, 0, 6); SB0();
    TMF(1, 0, 1); SB0();
    WLD(wf[10], vL, ws[0] + 0 * WP, 2048); SB0();
    TMF(1, 1, 1); SB0();
    asm volatile("" : XC_(xt[0][0][0][1]), XC_(xt[0][0][1][1])); SB0();
    MM(aP[0][0][1], wf[0], xt[0][0][0][1], Z4); SB0();
    TCV(3, 1, 0); SB0();
    TCV(0, 1, 1); SB0();
    TWR(3, 2, 1, 0); SB0();
    MM(aQ[0][0][1], wf[1], xt[0][0][1][1], Z4); SB0();
    WLD(wf[11], vL, ws[0] + 0 * WP, 3072); SB0();
    TCV(1, 1, 2); SB0();
    TWR(0, 2, 1, 1); SB0();
    MM(aI[0][0][1], wf[0], xt[0][0][1][1], Z4); SB0();
    TCV(2, 1, 3); SB0();
    TWR(1, 2, 1, 2); SB0();
    TCV(3, 1, 4); SB0();
    MM(aI[0][0][1], wf[1], xt[0][0][0][1], aI[0][0][1]); SB0();
    TWR(2, 2, 1, 3); SB0();
    TCV(0, 1, 5); SB0();
    TWR(3, 2, 1, 4); SB0();
    TMF(2, 0, 0); SB0();
    WLD(wf[12], vL, ws[1] + 0 * WP, 2048); SB0();
    TCV(1, 1, 6); SB0();
    TWR(0, 2, 1, 5); SB0();
    TMF(2, 1, 0); SB0();
    TWR(1, 2, 1, 6); SB0();
    TMF(2, 0, 1); SB0();
    TMF(2, 1, 1); SB0();
    WLD(wf[13], vL, ws[1] + 0 * WP, 3072); SB0();
    WAITV(10, wf[2], wf[3]); SB0();
    asm volatile("" : XC_(xt[0][1][0][0]), XC_(xt[0][1][1][0])); SB0();
    MM(aP[0][1][0], wf[2], xt[0][1][0][0], Z4); SB0();
    TCV(2, 2, 0); SB0();
    TCV(3, 2, 1); SB0();
    TWR(2, 2, 2, 0); SB0();
    MM(aQ[0][1][0], wf[3], xt[0][1][1][0], Z4); SB0();
    TCV(0, 2, 2); SB0();
    TWR(3, 2, 2, 1); SB0();
    TCV(1, 2, 3); SB0();
    MM(aI[0][1][0], wf[2], xt[0][1][1][0], Z4); SB0();
    WLD(wf[14], vL, ws[0] + 1 * WP, 0); SB0();
    TWR(0, 2, 2, 2); SB0();
    TCV(2, 2, 4); SB0();
    MM(aI[0][1][0], wf[3], xt[0][1][0][0], aI[0][1][0]); SB0();
    TWR(1, 2, 2, 3); SB0();
    TCV(3, 2, 5); SB0();
    TWR(2, 2, 2, 4); SB0();
    TMF(3, 0, 0); SB0();
    TCV(0, 2, 6); SB0();
    TWR(3, 2, 2, 5); SB0();
    TWR(0, 2, 2, 6); SB0();
    TMF(3, 1, 0); SB0();
    WLD(wf[15], vL, ws[0] + 1 * WP, 1024); SB0();
    TMF(3, 0, 1); SB0();
    TMF(3, 1, 1); SB0();
    WAITV(10, wf[4], wf[5]); SB0();
    asm volatile("s_waitcnt lgkmcnt(15)" : XC_(xt[1][0][0][0]), XC_(xt[1][0][1][0]) :: "memory"); SB0();
    MM(aP[0][0][0], wf[4], xt[1][0][0][0], aP[0][0][0]); SB0();
    WLD(wf[16], vL, ws[0] + 1 * WP, 2048); SB0();
    TCV(1, 3, 0); SB0();
    TCV(2, 3, 1); SB0();
    MM(aQ[0][0][0], wf[5], xt[1][0][1][0], aQ[0][0][0]); SB0();
    TWR(1, 2, 3, 0); SB0();
    TCV(3, 3, 2); SB0();
    TWR(2, 2, 3, 1); SB0();
    MM(aI[0][0][0], wf[4], xt[1][0][1][0], aI[0][0][0]); SB0();
    TCV(0, 3, 3); SB0();
    TWR(3, 2, 3, 2); SB0();
    TCV(1, 3, 4); SB0();
    MM(aI[0][0][0], wf[5], xt[1][0][0][0], aI[0][0][0]); SB0();
    WLD(wf[17], vL, ws[0] + 1 * WP, 3072); SB0();
    TWR(0, 2, 3, 3); SB0();
    TCV(2, 3, 5); SB0();
    asm volatile("" : XC_(xt[1][0][0][1]), XC_(xt[1][0][1][1])); SB0();
    MM(aP[0][0][1], wf[4], xt[1][0][0][1], aP[0][0][1]); SB0();
    TWR(1, 2, 3, 4); SB0();
    TCV(3, 3, 6); SB0();
    TWR(2, 2, 3, 5); SB0();
    MM(aQ[0][0][1], wf[5], xt[1][0][1][1], aQ[0][0][1]); SB0();
    TWR(3, 2, 3, 6); SB0();
    MM(aI[0][0][1], wf[4], xt[1][0][1][1], aI[0][0][1]); SB0();
    WLD(wf[18], vL, ws[1] + 1 * WP, -2048); SB0();
    MM(aI[0][0][1], wf[5], xt[1][0][0][1], aI[0][0][1]); SB0();
    asm volatile("s_waitcnt lgkmcnt(0)" ::: "memory");
    BAR(); SB0();
    STAMP(4);
    XRD(2, 0, 0, 0);
    XRD(2, 0, 1, 0);
    XRD(2, 0, 0, 1);
    XRD(2, 0, 1, 1);
    XRD(2, 1, 0, 0);
    XRD(2, 1, 1, 0);
    SB0();
    WAITV(11, wf[6], wf[7]); SB0();
    asm volatile("" : XC_(xt[1][1][0][0]), XC_(xt[1][1][1][0])); SB0();
    MM(aP[0][1][0], wf[6], xt[1][1][0][0], aP[0][1][0]); SB0();
    MM(aQ[0][1][0], wf[7], xt[1][1][1][0], aQ[0][1][0]); SB0();
    WLD(wf[19], vL, ws[1] + 1 * WP, -1024); SB0();
    MM(aI[0][1][0], wf[6], xt[1][1][1][0], aI[0][1][0]); SB0();
    MM(aI[0][1][0], wf[7], xt[1][1][0][0], aI[0][1][0]); SB0();
    WAITV(10, wf[8], wf[9]); SB0();
    MM(aP[1][0][0], wf[8], xt[0][0][0][0], Z4); SB0();
    WLD(wf[20], vL, ws[1] + 1 * WP, 0); SB0();
    MM(aQ[1][0][0], wf[9], xt[0][0][1][0], Z4); SB0();
    MM(aI[1][0][0], wf[8], xt[0][0][1][0], Z4); SB0();
    MM(aI[1][0][0], wf[9], xt[0][0][0][0], aI[1][0][0]); SB0();
    WLD(wf[21], vL, ws[1] + 1 * WP, 1024); SB0();
    MM(aP[1][0][1], wf[8], xt[0][0][0][1], Z4); SB0();
    MM(aQ[1][0][1], wf[9], xt[0][0][1][1], Z4); SB0();
    MM(aI[1][0][1], wf[8], xt[0][0][1][1], Z4); SB0();
    WLD(wf[22], vL, ws[1] + 1 * WP, 2048); SB0();
    MM(aI[1][0][1], wf[9], xt[0][0][0][1], aI[1][0][1]); SB0();
    WAITV(11, wf[10], wf[11]); SB0();
    asm volatile("s_waitcnt lgkmcnt(4)" : XC_(xt[2][0][0][0]), XC_(xt[2][0][1][0]) :: "memory"); SB0();
    MM(aP[0][0][0], wf[10], xt[2][0][0][0], aP[0][0][0]); SB0();
    MM(aQ[0][0][0], wf[11], xt[2][0][1][0], aQ[0][0][0]); SB0();
    WLD(wf[23], vL, ws[1] + 1 * WP, 3072); SB0();
    MM(aI[0][0][0], wf[10], xt[2][0][1][0], aI[0][0][0]); SB0();
    MM(aI[0][0][0], wf[11], xt[2][0][0][0], aI[0][0][0]); SB0();
    asm volatile("s_waitcnt lgkmcnt(2)" : XC_(xt[2][0][0][1]), XC_(xt[2][0][1][1]) :: "memory"); SB0();
    MM(aP[0][0][1], wf[10], xt[2][0][0][1], aP[0][0][1]); SB0();
    WLD(wf[24], vL, ws[0] + 2 * WP, -2048); SB0();
    MM(aQ[0][0][1], wf[11], xt[2][0][1][1], aQ[0][0][1]); SB0();
    ZC(0, 0, 0, 0); SB0();
    MM(aI[0][0][1], wf[10], xt[2][0][1][1], aI[0][0][1]); SB0();
    ZC(0, 0, 0, 1); SB0();
    MM(aI[0][0][1], wf[11], xt[2][0][0][1], aI[0][0][1]); SB0();
    WLD(wf[25], vL, ws[0] + 2 * WP, -1024); SB0();
    ZC(0, 0, 0, 2); SB0();
    WAITV(12, wf[12], wf[13]); SB0();
    asm volatile("s_waitcnt lgkmcnt(0)" : XC_(xt[2][1][0][0]), XC_(xt[2][1][1][0]) :: "memory"); SB0();
    MM(aP[0][1][0], wf[12], xt[2][1][0][0], aP[0][1][0]); SB0();
    ZC(0, 0, 0, 3); SB0();
    ZW(0, 0, 0); SB0();
    MM(aQ[0][1][0], wf[13], xt[2][1][1][0], aQ[0][1][0]); SB0();
    ZC(0, 0, 1, 0); SB0();
    MM(aI[0][1][0], wf[12], xt[2][1][1][0], aI[0][1][0]); SB0();
    WLD(wf[26], vL, ws[0] + 2 * WP, 0); SB0();
    ZC(0, 0, 1, 1); SB0();
    MM(aI[0][1][0], wf[13], xt[2][1][0][0], aI[0][1][0]); SB0();
    ZC(0, 0, 1, 2); SB0();
    WAITV(11, wf[14], wf[15]); SB0();
    MM(aP[1][0][0], wf[14], xt[1][0][0][0], aP[1][0][0]); SB0();
    ZC(0, 0, 1, 3); SB0();
    ZW(0, 0, 1); SB0();
    MM(aQ[1][0][0], wf[15], xt[1][0][1][0], aQ[1][0][0]); SB0();
    WLD(wf[27], vL, ws[0] + 2 * WP, 1024); SB0();
    ZC(0, 1, 0, 0); SB0();
    MM(aI[1][0][0], wf[14], xt[1][0][1][0], aI[1][0][0]); SB0();
    ZC(0, 1, 0, 1); SB0();
    MM(aI[1][0][0], wf[15], xt[1][0][0][0], aI[1][0][0]); SB0();
    ZC(0, 1, 0, 2); SB0();
    MM(aP[1][0][1], wf[14], xt[1][0][0][1], aP[1][0][1]); SB0();
    WLD(wf[28], vL, ws[0] + 2 * WP, 2048); SB0();
    ZC(0, 1, 0, 3); SB0();
    MM(aQ[1][0][1], wf[15], xt[1][0][1][1], aQ[1][0][1]); SB0();
    ZW(0, 1, 0); SB0();
    MM(aI[1][0][1], wf[14], xt[1][0][1][1], aI[1][0][1]); SB0();
    MM(aI[1][0][1], wf[15], xt[1][0][0][1], aI[1][0][1]); SB0();
    WLD(wf[29], vL, ws[0] + 2 * WP, 3072); SB0();
    STAMP(12);
    LGW0(); BAR(); SB0();
    STAMP(13);
    TABI();
    ZRD(0, 0); ZRD(1, 2 * M_ZT); SB0();
    WAITV(12, wf[16], wf[17]); SB0();
    MM(aP[1][0][0], wf[16], xt[2][0][0][0], aP[1][0][0]); SB0();
    MM(aQ[1][0][0], wf[17], xt[2][0][1][0], aQ[1][0][0]); SB0();
    MM(aI[1][0][0], wf[16], xt[2][0][1][0], aI[1][0][0]); SB0();
    WLD(wf[30], vL, ws[1] + 2 * WP, -2048); SB0();
    MM(aI[1][0][0], wf[17], xt[2][0][0][0], aI[1][0][0]); SB0();
    ZIW(0, 4); SB0();
    asm volatile("" : XC_(fi[0])); SB0();
    ZIM(0, 0); SB0();
    ZIM(0, 1); SB0();
    WLD(wf[31], vL, ws[1] + 2 * WP, -1024); SB0();
    ZC(1, 0, 0, 0); SB0();
    ZIW(1, 0); SB0();
    ZIM(1, 0); SB0();
    ZC(1, 0, 0, 1); SB0();
    ZIM(1, 1); SB0();
    ZC(1, 0, 0, 2); SB0();
    MM(aP[1][0][1], wf[16], xt[2][0][0][1], aP[1][0][1]); SB0();
    WLD(wf[32], vL, ws[1] + 2 * WP, 0); SB0();
    MM(aQ[1][0][1], wf[17], xt[2][0][1][1], aQ[1][0][1]); SB0();
    ZIC(0, 0); SB0();
    MM(aI[1][0][1], wf[16], xt[2][0][1][1], aI[1][0][1]); SB0();
    ZIC(0, 1); SB0();
    MM(aI[1][0][1], wf[17], xt[2][0][0][1], aI[1][0][1]); SB0();
    WLD(wf[33], vL, ws[1] + 2 * WP, 1024); SB0();
    ZIC(0, 2); SB0();
    WAITV(14, wf[18], wf[19]); SB0();
    MM(aP[1][1][0], wf[18], xt[0][1][0][0], Z4); SB0();
    YST(zo, ys + 0 * YT + 0 * YG); SB0();
    ZIC(1, 0); SB0();
    MM(aQ[1][1][0], wf[19], xt[0][1][1][0], Z4); SB0();
    ZIC(1, 1); SB0();
    MM(aI[1][1][0], wf[18], xt[0][1][1][0], Z4); SB0();
    WLD(wf[34], vL, ws[1] + 2 * WP, 2048); SB0();
    ZIC(1, 2); SB0();
    MM(aI[1][1][0], wf[19], xt[0][1][0][0], aI[1][1][0]); SB0();
    YST(zo, ys + 2 * YT + 0 * YG); SB0();
    ZC(1, 0, 0, 3); SB0();
    WAITV(15, wf[20], wf[21]); SB0();
    MM(aP[1][1][0], wf[20], xt[1][1][0][0], aP[1][1][0]); SB0();
    ZW(1, 0, 0); SB0();
    ZC(1, 0, 1, 0); SB0();
    MM(aQ[1][1][0], wf[21], xt[1][1][1][0], aQ[1][1][0]); SB0();
    WLD(wf[35], vL, ws[1] + 2 * WP, 3072); SB0();
    ZC(1, 0, 1, 1); SB0();
    MM(aI[1][1][0], wf[20], xt[1][1][1][0], aI[1][1][0]); SB0();
    ZC(1, 0, 1, 2); SB0();
    MM(aI[1][1][0], wf[21], xt[1][1][0][0], aI[1][1][0]); SB0();
    ZC(1, 0, 1, 3); SB0();
    ZW(1, 0, 1); SB0();
    WAITV(14, wf[22], wf[23]); SB0();
    MM(aP[1][1][0], wf[22], xt[2][1][0][0], aP[1][1][0]); SB0();
    MM(aQ[1][1][0], wf[23], xt[2][1][1][0], aQ[1][1][0]); SB0();
    MM(aI[1][1][0], wf[22], xt[2][1][1][0], aI[1][1][0]); SB0();
    MM(aI[1][1][0], wf[23], xt[2][1][0][0], aI[1][1][0]); SB0();
    WAITV(12, wf[24], wf[25]); SB0();
    MM(aP[2][0][0], wf[24], xt[0][0][0][0], Z4); SB0();
    MM(aQ[2][0][0], wf[25], xt[0][0][1][0], Z4); SB0();
    ZC(1, 1, 0, 0); SB0();
    MM(aI[2][0][0], wf[24], xt[0][0][1][0], Z4); SB0();
    ZC(1, 1, 0, 1); SB0();
    MM(aI[2][0][0], wf[25], xt[0][0][0][0], aI[2][0][0]); SB0();
    ZC(1, 1, 0, 2); SB0();
    MM(aP[2][0][1], wf[24], xt[0][0][0][1], Z4); SB0();
    ZC(1, 1, 0, 3); SB0();
    ZW(1, 1, 0); SB0();
    MM(aQ[2][0][1], wf[25], xt[0][0][1][1], Z4); SB0();
    MM(aI[2][0][1], wf[24], xt[0][0][1][1], Z4); SB0();
    MM(aI[2][0][1], wf[25], xt[0][0][0][1], aI[2][0][1]); SB0();
    STAMP(14);
    LGW0(); BAR(); SB0();
    STAMP(15);
    ZRD(0, M_Z1 + 0); ZRD(1, M_Z1 + 2 * M_ZT); SB0();
    WAITV(10, wf[26], wf[27]); SB0();
    MM(aP[2][0][0], wf[26], xt[1][0][0][0], aP[2][0][0]); SB0();
    MM(aQ[2][0][0], wf[27], xt[1][0][1][0], aQ[2][0][0]); SB0();
    MM(aI[2][0][0], wf[26], xt[1][0][1][0], aI[2][0][0]); SB0();
    MM(aI[2][0][0], wf[27], xt[1][0][0][0], aI[2][0][0]); SB0();
    ZIW(0, 4); SB0();
    ZIM(0, 0); SB0();
    VMW(1); ARRY(0); SB0();
    ZIM(0, 1); SB0();
    ZIW(1, 0); SB0();
    ZIM(1, 0); SB0();
    ZIM(1, 1); SB0();
    MM(aP[2][0][1], wf[26], xt[1][0][0][1], aP[2][0][1]); SB0();
    MM(aQ[2][0][1], wf[27], xt[1][0][1][1], aQ[2][0][1]); SB0();
    ZIC(0, 0); SB0();
    MM(aI[2][0][1], wf[26], xt[1][0][1][1], aI[2][0][1]); SB0();
    ZIC(0, 1); SB0();
    MM(aI[2][0][1], wf[27], xt[1][0][0][1], aI[2][0][1]); SB0();
    ZIC(0, 2); SB0();
    WAITV(9, wf[28], wf[29]); SB0();
    MM(aP[2][0][0], wf[28], xt[2][0][0][0], aP[2][0][0]); SB0();
    YST(zo, ys + 0 * YT + 1 * YG); SB0();
    ZIC(1, 0); SB0();
    MM(aQ[2][0][0], wf[29], xt[2][0][1][0], aQ[2][0][0]); SB0();
    ZIC(1, 1); SB0();
    MM(aI[2][0][0], wf[28], xt[2][0][1][0], aI[2][0][0]); SB0();
    ZIC(1, 2); SB0();
    MM(aI[2][0][0], wf[29], xt[2][0][0][0], aI[2][0][0]); SB0();
    YST(zo, ys + 2 * YT + 1 * YG); SB0();
    MM(aP[2][0][1], wf[28], xt[2][0][0][1], aP[2][0][1]); SB0();
    MM(aQ[2][0][1], wf[29], xt[2][0][1][1], aQ[2][0][1]); SB0();
    ZC(2, 0, 0, 0); SB0();
    MM(aI[2][0][1], wf[28], xt[2][0][1][1], aI[2][0][1]); SB0();
    ZC(2, 0, 0, 1); SB0();
    MM(aI[2][0][1], wf[29], xt[2][0][0][1], aI[2][0][1]); SB0();
    ZC(2, 0, 0, 2); SB0();
    WAITV(9, wf[30], wf[31]); SB0();
    MM(aP[2][1][0], wf[30], xt[0][1][0][0], Z4); SB0();
    ZC(2, 0, 0, 3); SB0();
    ZW(2, 0, 0); SB0();
    MM(aQ[2][1][0], wf[31], xt[0][1][1][0], Z4); SB0();
    ZC(2, 0, 1, 0); SB0();
    MM(aI[2][1][0], wf[30], xt[0][1][1][0], Z4); SB0();
    ZC(2, 0, 1, 1); SB0();
    MM(aI[2][1][0], wf[31], xt[0][1][0][0], aI[2][1][0]); SB0();
    ZC(2, 0, 1, 2); SB0();
    SB0();
    WAITV(7, wf[32], wf[33]); SB0();
    MM(aP[2][1][0], wf[32], xt[1][1][0][0], aP[2][1][0]); SB0();
    ZC(2, 0, 1, 3); SB0();
    ZW(2, 0, 1); SB0();
    MM(aQ[2][1][0], wf[33], xt[1][1][1][0], aQ[2][1][0]); SB0();
    MM(aI[2][1][0], wf[32], xt[1][1][1][0], aI[2][1][0]); SB0();
    MM(aI[2][1][0], wf[33], xt[1][1][0][0], aI[2][1][0]); SB0();
    WAITV(3, wf[34], wf[35]); SB0();
    MM(aP[2][1][0], wf[34], xt[2][1][0][0], aP[2][1][0]); SB0();
    MM(aQ[2][1][0], wf[35], xt[2][1][1][0], aQ[2][1][0]); SB0();
    MM(aI[2][1][0], wf[34], xt[2][1][1][0], aI[2][1][0]); SB0();
    MM(aI[2][1][0], wf[35], xt[2][1][0][0], aI[2][1][0]); SB0();
    STAMP(5);
    ZC(2, 1, 0, 0); SB0();
    ZC(2, 1, 0, 1); SB0();
    ZC(2, 1, 0, 2); SB0();
    ZC(2, 1, 0, 3); SB0();
    ZW(2, 1, 0); SB0();
    VMW(0); ARRY(1); SB0();
    STAMP(1);
    SB0();
    STAMP(2);
    LGW0(); BAR(); SB0();
    }
  }
  STAMP(6);
  ZRD(0, 2 * M_Z1); ZRD(1, 2 * M_Z1 + 2 * M_ZT);
  SB0();
  asm volatile("" : "+v"(vL));
  const unsigned z0 = min(wave * 16u + (vL >> 5) * 128u, 256u) + (vL & 16u) / 2;
  POLLM(z0, flc, 0x3full, " sc1");
  lane = vL >> 4; g = lane >> 4; r = lane & 15; q = (lane >> 2) & 3; p = lane & 3;
  ZINV2(2);
  if (L >= I_NWG) { VMW(0); ARRY(2); STAMP_END(11); return; }
  TABG();
  unsigned long long miss;
  asm volatile("s_mov_b64 exec, 0x3f\n\ts_waitcnt vmcnt(2)\n\tv_cmp_ne_u64 %0, %2, %1\n\ts_mov_b64 exec, -1\n\ts_nop 3" : "=s"(miss), "+v"(pv) : "s"(tag) : "memory");
  if (__builtin_expect(miss == 0, 1)) { IDMA(0); VMW(2); ARRY(2); POLL(1); STAMP(3); VMW(1); }
  else { VMW(0); ARRY(2); STAMP(3); CHECKM(1, z0, flc, 0x3full, POLL_SLEEP); IDMA(0); POLL(1); VMW(0); }
  asm volatile("s_waitcnt lgkmcnt(0)" : "+v"(gi[0][0]), "+v"(gi[0][1]), "+v"(gi[1][0]), "+v"(gi[1][1]) :: "memory");
  if (wave == 7) CHECK(0, 1);
  const int ifw = 2 * g + (q >> 1);
  const unsigned ia = sbase + M_RA + (p >> 1) * 512 + (q & 1) * 32 + (p & 1) * 16;
  const unsigned i0 = ia + ifw * 1024 + ((wave ^ (ifw & 3)) << 6), is = ia + 16 * 1024 + (wave << 6);
  const rsrc_t rs = make_rsrc(outp, IMG * IMG * CH * 2);
  const int tq = ((L >> 2) * 37) >> 8;
  const int px0 = (32 * ((L >> 2) - 7 * tq) + 8 * (L & 3) + wave) * IMG + 28 * tq;
  const unsigned vo = (r < 14) ? (unsigned)((px0 + r) * (CH * 2) + g * 16) : OOB;
  constexpr unsigned C14H = split_hl(1.0 / 14.0, 0), C14L = split_hl(1.0 / 14.0, 1);
  const u32x4 se[2] = {u32x4{0u, 0u, (lane == 0) ? C14H : 0u, (lane == 0) ? C14L : 0u}, u32x4{0u, 0u, (lane == 13) ? C14H << 16 : 0u, (lane == 13) ? C14L << 16 : 0u}};
  u32x2 id[2][2][3];
  u32x4 io[2][2];
#define IRD(s) do { _Pragma("unroll") for (int t_ = 0; t_ < 2; ++t_) { \
    TRR(id[(s) & 1][t_][0], i0, (s) * YS + t_ * 8); TRR(id[(s) & 1][t_][1], i0, (s) * YS + t_ * 8 + 8192); TRR(id[(s) & 1][t_][2], is, (s) * YS + t_ * 8); } } while (0)
#define IINV(s, n) do { u32x2 (&d_)[2][3] = id[(s) & 1]; \
    asm volatile("s_waitcnt lgkmcnt(%0)" :: "i"(n) : "memory"); SB0(); \
    u32x6 v_[2] = {u32x6{d_[0][0][0], d_[0][0][1], d_[0][1][0], d_[0][1][1], d_[0][2][0], d_[0][2][1]}, \
                   u32x6{d_[1][0][0], d_[1][0][1], d_[1][1][0], d_[1][1][1], d_[1][2][0], d_[1][2][1]}}; \
    asm volatile("" : "+v"(v_[0]), "+v"(v_[1])); \
    _Pragma("unroll") for (int pt = 0; pt < 2; ++pt) { f32x4 e_[2]; \
      _Pragma("unroll") for (int t_ = 0; t_ < 2; ++t_) { const u32x4 a_ = __builtin_shufflevector(v_[t_], v_[t_], 0, 1, 2, 3); \
        e_[t_] = mfma32(a_, gi[pt][0], mfma32(a_, gi[pt][1], mfma32(__builtin_shufflevector(v_[t_], v_[t_], 2, 3, 4, 5), se[pt], Z4))); } \
      io[(s) & 1][pt] = pack8(e_[0], e_[1]); } } while (0)
#define IST(s) do { __builtin_amdgcn_raw_buffer_store_b128(io[(s) & 1][0], rs, vo + (s) * 64, 0, ST_AUX); \
    __builtin_amdgcn_raw_buffer_store_b128(io[(s) & 1][1], rs, vo + (s) * 64 + 14 * CH * 2, 0, ST_AUX); } while (0)
  STAMP(7);
  BAR();
  IDMA(1); POLL(2);
  IRD(0); IINV(0, 0);
  if (wave < NEW) { IST(0); VMW(2); }
  else CHECK(0, 2);
  STAMP(8);
  BAR();
  IDMA(2);
  if (wave < NEW) { IRD(1); IINV(1, 0); IST(1); VMW(2); }
  else { IST(0); IRD(1); IINV(1, 0); IST(1); VMW(4); }
  STAMP(9);
  BAR();
  IRD(2);
  IINV(2, 0); IST(2);
  asm volatile("fftconv_code_end:");
  STAMP_END(11);
}

extern "C" void kernel_launch(void* const* d_in, const int* in_sizes, int n_in,
                              void* d_out, int out_size, void* d_ws, size_t ws_size,
                              hipStream_t stream) {
  const char* x = (const char*)d_in[0];
  const char* w = (const char*)d_in[1];
  char* ws = (char*)d_ws;
  k_conv<<<dim3(F_NWG), dim3(512), 0, stream>>>(x, w, ws, (char*)d_out INSTR_ARG(0));
}
```

```cpp
#include <hip/hip_runtime.h>
#include <hip/hip_bf16.h>

typedef __bf16 bf16x8 __attribute__((ext_vector_type(8)));
typedef __bf16 bf16x2 __attribute__((ext_vector_type(2)));
typedef float  f32x4  __attribute__((ext_vector_type(4)));
typedef unsigned u32x2 __attribute__((ext_vector_type(2)));
typedef unsigned u32x4 __attribute__((ext_vector_type(4)));
typedef unsigned u32x6 __attribute__((ext_vector_type(6)));
typedef unsigned u32x8 __attribute__((ext_vector_type(8)));
typedef unsigned u32x10 __attribute__((ext_vector_type(10)));

#define LDS3 __attribute__((address_space(3)))

#define INSTR_PARAM
#define INSTR_ARG(k)
#define INSTR_DECL
#define STAMP(i)
#define CYC0()
#define CYC1()
#define STAMP_END(i)
#define STAMP_MID()
#define INSTR_LDS 0

constexpr int IMG = 224, CH = 96;
constexpr int NFW = 17;

constexpr int    XT = 224 * 64, XC = 8 * XT, XF = 2 * XC;
constexpr size_t XW_OFF = (size_t)36 << 20;
constexpr int    XC2 = 2 * XC, XF2 = 1 << 20;
constexpr size_t WT_OFF = 0;
constexpr int    WK = 16384, WP = 14 * WK, WH = 1 << 20, WF = 2 * WH;
constexpr size_t YH_OFF = (size_t)96 << 20;
constexpr int    YS = NFW * 1024, YG = 3 * 16384, YT = 3 * YG, YQ = 1 << 20;
constexpr size_t CN_OFF = (size_t)34 << 20;
constexpr int    FL_W = 896 * 128, FL_Y = FL_W + 288 * 128;
constexpr int    CN_G = 384, CN_P = 56 * CN_G, FL_V = FL_Y + 3 * CN_P;
constexpr size_t EP_OFF = (size_t)32 << 20 | 0xc0000;
constexpr unsigned long long EP_STEP = 0x9e3779b97f4a7c15ull;

struct CosTab { double v[448]; };
constexpr CosTab make_cos() {
  CosTab t{};
  const double pi = 3.14159265358979323846264338327950288;
  for (int k = 0; k <= 112; ++k) {
    const double a = 2.0 * pi * k / 448.0;
    double term = 1.0, sum = 1.0;
    for (int n = 1; n <= 14; ++n) { term *= -a * a / ((2.0 * n - 1.0) * (2.0 * n)); sum += term; }
    if (k == 112) sum = 0.0;
    t.v[k] = sum; t.v[(448 - k) % 448] = sum; t.v[224 - k] = -sum; t.v[224 + k] = -sum;
  }
  return t;
}
constexpr CosTab kCos = make_cos();
constexpr double cs(int idx) { return kCos.v[(idx + 448) % 448]; }
constexpr unsigned short bf16_rne(float f) {
  unsigned u = __builtin_bit_cast(unsigned, f);
  u += 0x7fffu + ((u >> 16) & 1u);
  return (unsigned short)(u >> 16);
}
constexpr float bf16_val(unsigned short b) { return __builtin_bit_cast(float, (unsigned)b << 16); }
constexpr unsigned short split_hl(double v, int hl) {
  const unsigned short hi = bf16_rne((float)v);
  return hl == 0 ? hi : bf16_rne((float)(v - (double)bf16_val(hi)));
}
struct FragD { unsigned short v[2][2][64][8]; };
struct TabM { unsigned short fh[2][2][2][64][8], fi[2][2][64][8], g[2][2][64][8]; FragD d; };
constexpr FragD make_D() {
  FragD t{};
  for (int ct = 0; ct < 2; ++ct) for (int l = 0; l < 64; ++l) for (int jj = 0; jj < 8; ++jj) {
    const int cp = 16 * ct + (l & 15), v = 8 * (l >> 4) + jj;
    const double e = cs(7 * (2 * (cp >> 1) + 1) * v + 112 * (cp & 1));
    for (int hl = 0; hl < 2; ++hl) t.v[ct][hl][l][jj] = split_hl(e, hl);
  }
  return t;
}
constexpr TabM make_M() {
  TabM t{};
  t.d = make_D();
  for (int s = 0; s < 2; ++s) for (int l = 0; l < 64; ++l) for (int jj = 0; jj < 8; ++jj) {
    const int n = l & 15, g = l >> 4, hh = jj >> 2, q = jj & 3;
    for (int ct = 0; ct < 2; ++ct) {
      const int kr = 16 * hh + 4 * g + q, a = kr >> 1, ri = kr & 1, cp = 4 * (4 * ct + (n & 3)) + (n >> 2), k = cp >> 1, ro = cp & 1;
      double e = 0.0;
      if (a < 14 && k < 14) e = (s == 0) ? cs(32 * k * a + 112 * (ro - ri)) : (ri == k / 7) ? cs(16 * (2 * (k % 7) + 1) * a + 112 * ro) : 0.0;
      for (int hl = 0; hl < 2; ++hl) t.fh[s][ct][hl][l][jj] = split_hl(e, hl);
    }
    const int k = 8 * hh + 2 * g + (q >> 1), ri = q & 1;
    {
      const int j = n >> 1, ro = n & 1;
      double e = 0.0;
      if (k < 14) e = (s == 0) ? cs(32 * k * (j + 6) + 112 * (ri - ro)) : (ro == k / 7) ? 2.0 * cs(16 * (2 * (k % 7) + 1) * (j + 6) + 112 * ri) : 0.0;
      for (int hl = 0; hl < 2; ++hl) t.fi[s][hl][l][jj] = split_hl(e, hl);
    }
    {
      const double e = (n < 14) ? cs(7 * (2 * k + 1) * (14 * s + n + 5) + 112 * ri) / 224.0 : 0.0;
      for (int hl = 0; hl < 2; ++hl) t.g[s][hl][l][jj] = split_hl(e, hl);
    }
  }
  return t;
}
__device__ __attribute__((aligned(4096))) char gXh[NFW * XF];
__device__ __attribute__((aligned(64))) const TabM kTabM = make_M();

__device__ __forceinline__ unsigned pack2(float a, float b) {
  bf16x2 v; v[0] = (__bf16)a; v[1] = (__bf16)b;
  return __builtin_bit_cast(unsigned, v);
}
__device__ __forceinline__ f32x4 mfma32(u32x4 a, u32x4 b, f32x4 c) {
  return __builtin_amdgcn_mfma_f32_16x16x32_bf16(__builtin_bit_cast(bf16x8, a), __builtin_bit_cast(bf16x8, b), c, 0, 0, 0);
}
__device__ __forceinline__ u32x4 cat(u32x2 a, u32x2 b) { return u32x4{a[0], a[1], b[0], b[1]}; }
__device__ __forceinline__ u32x4 pack8(f32x4 a, f32x4 b) { return u32x4{pack2(a[0], a[1]), pack2(a[2], a[3]), pack2(b[0], b[1]), pack2(b[2], b[3])}; }
#define Z4 (f32x4{0.f, 0.f, 0.f, 0.f})
#define ST_AUX 16
typedef __amdgpu_buffer_rsrc_t rsrc_t;
__device__ __forceinline__ rsrc_t make_rsrc(void* p, unsigned bytes) { return __builtin_amdgcn_make_buffer_rsrc(p, 0, bytes, 0x00020000); }
constexpr unsigned OOB = 0x80000000u;
#define SB0() __builtin_amdgcn_sched_barrier(0)
#define BAR() __builtin_amdgcn_s_barrier()
#define VMW(n) asm volatile("s_waitcnt vmcnt(%0)" :: "i"(n) : "memory")
#define TRR(dst, addr, off) asm volatile("ds_read_b64_tr_b16 %0, %1 offset:%2" : "=v"(dst) : "v"(addr), "i"(off) : "memory")
#define GLD(dst, voff, sb, imm) asm volatile("global_load_dwordx4 %0, %1, %2 offset:%3" : "=v"(dst) : "v"(voff), "s"(sb), "i"(imm) : "memory")
#define LD_SC " sc1"
#define GLD1(dst, voff, sb, imm) asm volatile("global_load_dwordx4 %0, %1, %2 offset:%3" LD_SC : "=v"(dst) : "v"(voff), "s"(sb), "i"(imm) : "memory")
#define DMA1(m0v, voff, sb, imm) asm volatile("s_mov_b32 m0, %0\n\ts_nop 0\n\tglobal_load_lds_dwordx4 %1, %2 offset:%3 sc1" :: "s"(m0v), "v"(voff), "s"(sb), "i"(imm) : "memory")
#define DMA(m0v, voff, sb, imm) asm volatile("s_mov_b32 m0, %0\n\ts_nop 0\n\tglobal_load_lds_dwordx4 %1, %2 offset:%3" :: "s"(m0v), "v"(voff), "s"(sb), "i"(imm) : "memory")
#define BST(v, voff, rs, so) asm volatile("buffer_store_dwordx4 %0, %1, %2, %3 offen sc1\n\ts_nop 1" :: "v"(v), "v"(voff), "s"(rs), "s"(so) : "memory")
#define WAIT2(n, a, b) asm volatile("s_waitcnt lgkmcnt(%2)" : "+v"(a), "+v"(b) : "i"(n) : "memory")
#define WAIT4(n, a, b, c, d) asm volatile("s_waitcnt lgkmcnt(%4)" : "+v"(a), "+v"(b), "+v"(c), "+v"(d) : "i"(n) : "memory")

#define TWL 0
#define XPR 2
#define WG_X 200
#define WG_W 200
constexpr int W_GATE = WG_X;
constexpr int W_SLOPE = 4, W_OFF = 290;
constexpr int F_RS = 768 * 16;
constexpr int F_WOFF = 4 * F_RS;
constexpr int F_CNT = 154112;
constexpr int F_NW0 = 32;
constexpr int F_NPF = 16;
constexpr int F_NWG = 256;

__device__ __forceinline__ int comp_of(int u, int n) { return n < 56 ? 56 * u + n : n < 60 ? 392 + 4 * u + n : 476; }
__device__ __forceinline__ void make_e(int cp, int g, u32x4 (&e)[2][2]) {
  const int fw = (cp * 2341) >> 16, rem = cp - 28 * fw, k = rem >> 1;
  const bool sp = fw >= 16;
  const float fa = sp ? 0.f : (float)(2 * fw + 1) * (1.f / 64.f);
  const float fb = (float)(sp ? 2 * (k >= 7 ? k - 7 : k) + 1 : 2 * k) * (1.f / 28.f);
#pragma unroll
  for (int ks = 0; ks < 2; ++ks) {
    const float f0 = fb * (float)(4 * ks + g) + ((rem & 1) ? 0.25f : 0.f);
    const bool ok = (ks == 0) || g < 3;
    const float m0 = (ok && (!sp || k >= 7)) ? 1.f : 0.f, m1 = (ok && !sp) ? 1.f : 0.f, m6 = (ok && (!sp || k < 7)) ? 1.f : 0.f;
    float v[8];
#pragma unroll
    for (int j = 0; j < 7; ++j) v[j] = __builtin_amdgcn_cosf(__builtin_fmaf(fa, (float)j, f0)) * (j == 0 ? m0 : j == 6 ? m6 : m1);
    v[7] = 0.f;
#pragma unroll
    for (int s = 0; s < 4; ++s) {
      const unsigned h = pack2(v[2 * s], v[2 * s + 1]);
      e[ks][0][s] = h;
      e[ks][1][s] = pack2(v[2 * s] - __builtin_bit_cast(float, h << 16), v[2 * s + 1] - __builtin_bit_cast(float, h & 0xffff0000u));
    }
  }
}

#define GATE(lim, slope, off) do { unsigned lim_; \
    asm volatile("s_memrealtime vcc\n\ts_waitcnt lgkmcnt(0)\n\ts_sub_u32 vcc_lo, vcc_lo, %1\n\ts_mul_i32 %0, vcc_lo, %3\n\ts_sub_i32 %0, %0, %4\n\ts_max_i32 %0, %0, 0\n\ts_min_u32 %0, %0, %2\n" \
                 "1:\n\ts_cmp_ge_u32 vcc_lo, %0\n\ts_cbranch_scc1 2f\n\ts_memrealtime vcc\n\ts_waitcnt lgkmcnt(0)\n\ts_sub_u32 vcc_lo, vcc_lo, %1\n\ts_branch 1b\n2:" \
                 : "=&s"(lim_) : "s"((unsigned)t0), "i"(lim), "i"(slope), "i"(off) : "memory", "vcc", "scc"); } while (0)
#define KPTR(i) ((char*)(((unsigned long long)ka[2 * (i) + 1] << 32) | ka[2 * (i)]))
#define WSP(i) ((char*)(((unsigned long long)KPTR(i) + 0x3fffffu) & ~0x3fffffull))
#define PLD() asm volatile("global_load_dwordx2 %0, %1, %2 offset:128 sc1" : "=v"(pw) : "v"((lane & 15) * 8), "s"(ep) : "memory")
#define PLC(n) do { unsigned st_; asm volatile("s_mov_b32 %[sp], 0\n\ts_waitcnt vmcnt(%[nn])\n" \
    "1:\n\tv_lshrrev_b64 %[t], 16, %[v]\n\tv_cmp_ne_u64 vcc, %[tg], %[t]\n\ts_nop 4\n\ts_cbranch_vccz 2f\n\t" \
    "s_add_u32 %[sp], %[sp], 1\n\ts_cmp_ge_u32 %[sp], %[lim]\n\ts_cbranch_scc1 2f\n\ts_sleep 2\n\t" \
    "global_load_dwordx2 %[v], %[z], %[p] offset:128 sc1\n\ts_waitcnt vmcnt(0)\n\ts_branch 1b\n2:" \
    : [v] "+v"(pw), [sp] "=&s"(st_), [t] "=&v"(pt) : [z] "v"((lane & 15) * 8), [p] "s"(ep), [tg] "s"(tag >> 16), [lim] "i"(SPIN_MAX), [nn] "i"(n) : "memory", "scc", "vcc"); \
    unsigned sm_ = pw[0] + __builtin_amdgcn_update_dpp(0u, pw[0], 0xb1, 0xf, 0xf, true); \
    sm_ += __builtin_amdgcn_update_dpp(0u, sm_, 0x4e, 0xf, 0xf, true); \
    const unsigned c0_ = __builtin_amdgcn_readlane(sm_, 8), c1_ = __builtin_amdgcn_readlane(sm_, 12); \
    sm_ += __builtin_amdgcn_update_dpp(0u, sm_, 0x141, 0xf, 0xf, true); \
    pl = __builtin_amdgcn_readfirstlane(((pw[0] >> 12) & 7u) | ((~sm_ >> 8) & 8u)) | ((~c0_ >> 6) & 16u) | ((~c1_ >> 5) & 32u); } while (0)
#define PMASK(pl_) ((0xf0f0u >> ((pl_) & 7)) ^ (0u - (((pl_) >> 3) & 1)))
constexpr int SPIN_MAX = 1 << 16;
#define LANDED(sh) do { unsigned n_; \
    asm volatile("s_waitcnt vmcnt(0)\n\ts_mov_b64 exec, %3\n\tds_add_u32 %1, %2\n\ts_mov_b64 exec, -1\n" \
                 "1:\n\tds_read_u8 %0, %1 offset:%4\n\ts_waitcnt lgkmcnt(0)\n\tv_cmp_gt_u32 vcc, 4, %0\n\ts_nop 4\n\ts_cbranch_vccnz 1b" \
                 : "=&v"(n_) : "v"(sbase + F_CNT), "v"(1u << (8 * (sh))), "s"((sh) ? (unsigned long long)(wave >> 2) : 1ull), "i"(sh) : "memory", "vcc"); SB0(); } while (0)
#define WDMA() do { if (wave >= 4) { const char* wgt = KPTR(1) + oW; \
    _Pragma("unroll") for (int i = 0; i < 7; ++i) DMA(sbase + F_WOFF + w4 * 1024 + i * 4096, wv[i & 1], wgt + i * (7 * CH * CH * 2), 0); } } while (0)
#define KWAIT() do { SB0(); asm volatile("s_waitcnt lgkmcnt(0)\n\ts_memrealtime %0" : "=s"(t0) :: "memory"); SB0(); asm volatile("" : "+s"(ka), "+s"(s3), "+s"(s4)); } while (0)

constexpr int M_XT = 1824;
constexpr int M_RA = 0, M_RB = 32 * M_XT;
constexpr int M_ZT = 912, M_Z1 = 32 * M_ZT;
constexpr int M_TAB = M_RB + 3 * M_Z1;
constexpr int M_LDS = F_CNT + 512 + INSTR_LDS;
static_assert(F_CNT == M_TAB + 8192, "");
constexpr int M_NI = NFW * 14, I_NWG = 224;
constexpr int POLL_SLEEP = 2;
#define H_LO 0

#define DSR(dst, addr, off) asm volatile("ds_read_b128 %0, %1 offset:%2" : "=v"(dst) : "v"(addr), "i"(off) : "memory")
#define DSW(addr, v, off) asm volatile("ds_write_b128 %0, %1 offset:%2" :: "v"(addr), "v"(v), "i"(off) : "memory")
#define WLD GLD1
#define XC_(r) "+v"(r)
#define PIN(r) asm volatile("" :: "v"(r))
#define MM(d, a, b, c) do { d = mfma32(a, b, c); PIN(d); } while (0)
#define WAITV(n, a, b) asm volatile("s_waitcnt vmcnt(%2)" : "+v"(a), "+v"(b) : "i"(n) : "memory")
#define LGW0() asm volatile("s_waitcnt lgkmcnt(0)" ::: "memory")
#define XLD(cc, chg, i) do { if ((i) < 2) GLD1(xr[cc][chg][(i) & 1], vL, (cc) ? xs2 + (2 * (cc) + (chg) - 2) * XC2 : xs + (chg) * XC, (i) * 1024); \
    else asm volatile("global_load_dwordx2 %0, %1, %2" LD_SC : "=v"(xh[cc][chg]) : "v"(vL), "s"((cc) ? xsh2 + (2 * (cc) + (chg) - 2) * XC2 : xsh + (chg) * XC) : "memory"); } while (0)
#define ARRIVE(p) asm volatile("s_mov_b64 exec, 1\n\tglobal_store_dwordx2 %0, %1, %2 sc1\n\ts_mov_b64 exec, -1" :: "v"(0u), "v"(tag), "s"(p) : "memory")
#define ARRIVEL(p) asm volatile("s_mov_b64 exec, 0xffff\n\tglobal_store_dwordx2 %0, %1, %2 sc1\n\ts_mov_b64 exec, -1" :: "v"(lane * 8), "v"(tag), "s"(p) : "memory")
#define POLLA(z_, p_, w_) asm volatile("s_cmp_lg_u32 %3, 7\n\ts_cbranch_scc1 2f\n\tglobal_load_dwordx2 %0, %1, %2 sc1\n2:" \
    : "=v"(pv) : "v"(z_), "s"(p_), "s"(w_) : "memory", "scc")
#define CHECKA(n, z_, p_, w_, sl_) asm volatile("s_mov_b32 %[sp], 0\n\ts_waitcnt vmcnt(%[nn])\n\ts_cmp_lg_u32 %[wv], 7\n\ts_cbranch_scc1 2f\n" \
    "1:\n\tv_cmp_ne_u64 vcc, %[tg], %[v]\n\ts_nop 4\n\ts_cbranch_vccz 2f\n\t" \
    "s_add_u32 %[sp], %[sp], 1\n\ts_cmp_ge_u32 %[sp], %[lim]\n\ts_cbranch_scc1 2f\n\ts_mov_b64 exec, vcc\n\ts_sleep %[sl]\n\t" \
    "global_load_dwordx2 %[v], %[z], %[p] sc1\n\ts_waitcnt vmcnt(0)\n\ts_branch 1b\n2:\n\ts_mov_b64 exec, -1" \
    : [v] "+v"(pv), [sp] "=&s"(st) : [z] "v"(z_), [p] "s"(p_), [tg] "s"(tag), [lim] "i"(SPIN_MAX), [nn] "i"(n), [sl] "i"(sl_), [wv] "s"(w_) : "memory", "scc", "vcc")
#define POLLM(z_, p_, m_, sc_) asm volatile("s_mov_b64 exec, %3\n\tglobal_load_dwordx2 %0, %1, %2" sc_ "\n\ts_mov_b64 exec, -1" : "=v"(pv) : "v"(z_), "s"(p_), "s"(m_) : "memory")
#define CHECKM(n, z_, p_, m_, sl_) asm volatile("s_mov_b32 %[sp], 0\n\ts_mov_b64 exec, %[m]\n\ts_waitcnt vmcnt(%[nn])\n" \
    "1:\n\tv_cmp_ne_u64 vcc, %[tg], %[v]\n\ts_nop 4\n\ts_cbranch_vccz 2f\n\t" \
    "s_add_u32 %[sp], %[sp], 1\n\ts_cmp_ge_u32 %[sp], %[lim]\n\ts_cbranch_scc1 2f\n\ts_mov_b64 exec, vcc\n\ts_sleep %[sl]\n\t" \
    "global_load_dwordx2 %[v], %[z], %[p] sc1\n\ts_waitcnt vmcnt(0)\n\ts_branch 1b\n2:\n\ts_mov_b64 exec, -1" \
    : [v] "+v"(pv), [sp] "=&s"(st) : [z] "v"(z_), [p] "s"(p_), [tg] "s"(tag), [lim] "i"(SPIN_MAX), [nn] "i"(n), [sl] "i"(sl_), [m] "s"(m_) : "memory", "scc", "vcc")
#define HOLD 2
#define P_GATE 0
#define F_SLEEP 2

constexpr int NEW = 5;

#define YST(v, sb, s_) asm volatile("global_store_dwordx4 %0, %1, %2 sc1\n\ts_nop 1" :: "v"(yv[s_]), "v"(v), "s"(sb) : "memory")
#define KOFF(Q_, row) ((co & 0x1fffffu) + (unsigned)(((32 * ((Q_) - 7 * (((Q_) * 37) >> 8)) + (row)) * IMG + 28 * (((Q_) * 37) >> 8) + 14) * (CH * 2)))
#define KROW(Q_, j) ((KOFF(Q_, j) >> 13) & 1u)
#define KTIL(Q_, tile) ({ const unsigned o_ = KOFF(Q_, 8 * (tile) + 4); ((o_ >> 21) ^ (co >> (30 + (o_ >> 23)))) & 1u; })

extern "C" __global__ __launch_bounds__(512, 2)
void k_conv(const char* __restrict__ x_, const char* __restrict__ wgt_, char* __restrict__ ws_, char* __restrict__ out_ INSTR_PARAM)
{
  __shared__ __attribute__((aligned(16))) char smem[M_LDS];
  INSTR_DECL;
  u32x8 ka;
  unsigned long long cb, tag, pt, t0;
  u32x2 pw;
  unsigned pl, tf, s3, s4;
  const unsigned sbase = (unsigned)(size_t)(LDS3 char*)smem;
  const int b = blockIdx.x;
  {
    unsigned s0, s1, s2, va, vb, vc, vd, ve, vf, vg;
    unsigned long long t2, t3, vz;
    u32x4 vq;
    asm volatile("s_load_dwordx8 %[ka], %[kp], 0\n\tv_readfirstlane_b32 %[tf], %[id]\n\t"
                 "s_add_i32 m0, %[sb], %[co]\n\tv_mov_b32 %[vb], 0\n\tv_mov_b32 %[va], m0\n\tds_write2_b32 %[va], %[vb], %[vb] offset1:1\n\tds_write_b32 %[va], %[vb] offset:8\n\t"
                 "s_cmp_lt_u32 %[b], 32\n\ts_cbranch_scc1 2f\n\t"
                 "s_and_b32 %[s0], %[b], 7\n\ts_mul_i32 %[s0], %[s0], 28\n\ts_lshr_b32 %[s1], %[b], 3\n\ts_add_i32 %[s0], %[s0], %[s1]\n\ts_add_i32 %[s0], %[s0], -4\n\t"
                 "v_and_b32 %[va], 63, %[id]\n\t"
                 "s_and_b32 %[s1], %[s0], -16\n\ts_lshr_b32 %[s2], %[s0], 1\n\ts_and_b32 %[s2], %[s2], 6\n\ts_add_i32 %[s1], %[s1], %[s2]\n\t"
                 "v_cmp_lt_u32 vcc, 31, %[va]\n\tv_lshlrev_b32 %[va], 4, %[va]\n\t"
                 "s_lshr_b32 %[s2], %[tf], 6\n\ts_mul_i32 %[s2], %[s2], 5\n\ts_and_b32 %[s2], %[s2], 9\n\ts_add_i32 %[s1], %[s1], %[s2]\n\t"
                 "v_xor_b32 %[vc], 32, %[va]\n\t"
                 "s_add_i32 %[s2], %[s1], -6\n\ts_addk_i32 %[s1], 218\n\ts_min_u32 %[s1], %[s1], %[s2]\n\t"
                 "s_and_b32 %[s0], %[s0], 3\n\ts_cselect_b32 %[s2], 0, 43008\n\t"
                 "v_cndmask_b32 %[vb], %[va], %[vc], vcc\n\tv_add_u32 %[vd], %[s2], %[va]\n\t"
                 "s_lshl2_add_u32 %[s1], %[s1], %[s0]\n\ts_mulk_i32 %[s1], 10752\n\ts_addk_i32 %[s1], 2560\n\t"
                 "v_cndmask_b32 %[ve], %[vd], %[vc], vcc\n\t"
                 "s_lshl_b32 %[s2], %[b], 3\n\ts_and_b32 %[s2], %[s2], 0xffffffc0\n\ts_addk_i32 %[s2], -256\n\t"
                 "v_lshrrev_b32 %[vf], 3, %[id]\n\tv_add_u32 %[vf], %[s2], %[vf]\n\tv_min_u32 %[vf], 1763, %[vf]\n\t"
                 "v_mul_u32_u24 %[vg], 10923, %[vf]\n\tv_lshrrev_b32 %[vg], 16, %[vg]\n\tv_mul_u32_u24 %[vg], 18, %[vg]\n\t"
                 "s_and_b32 %[s2], %[b], 3\n\ts_mul_i32 %[s2], %[s2], 6\n\tv_add3_u32 %[vf], %[vf], %[vg], %[s2]\n\tv_lshlrev_b32 %[vf], 7, %[vf]\n\t"
                 "s_lshl_b32 %[s3], %[b], 9\n\ts_add_i32 %[s3], %[s3], %[tf]\n\ts_lshl1_add_u32 %[s3], %[s3], %[fo]\n\t"
                 "s_addk_i32 m0, 256\n\ts_mul_i32 %[s0], %[tf], 192\n\t"
                 "s_waitcnt lgkmcnt(0)\n\tglobal_load_lds_dword %[vf], s[14:15]\n\t"
                 "s_add_u32 vcc_lo, s16, 0x3fffff\n\ts_addc_u32 vcc_hi, s17, 0\n\ts_and_b32 vcc_lo, vcc_lo, 0xffc00000\n\ts_mov_b32 %[s2], %[eo]\n\t"
                 "s_load_dwordx2 %[tg], vcc, %[s2]\n\ts_load_dword %[s4], vcc, %[s3] offset:64\n\ts_load_dword %[s3], vcc, %[s3]\n\ts_add_i32 m0, %[s0], %[sb]\n\t"
                 "s_barrier\n\ts_cmpk_ge_u32 %[tf], 0x100\n\ts_cbranch_scc1 1f\n\ts_setprio 3\n\ts_memrealtime %[t0]\n\t"
                 "s_mov_b64 vcc, s[12:13]\n\ts_add_u32 vcc_lo, vcc_lo, %[s1]\n\ts_addc_u32 vcc_hi, vcc_hi, 0\n\t"
                 "global_load_lds_dwordx4 %[vd], vcc offset:-4096\n\tglobal_load_lds_dwordx4 %[ve], vcc offset:-3072\n\t"
                 "global_load_lds_dwordx4 %[vc], vcc offset:-2048\n\tglobal_load_lds_dwordx4 %[va], vcc offset:-1024\n\t"
                 "global_load_lds_dwordx4 %[vb], vcc\n\tglobal_load_lds_dwordx4 %[vc], vcc offset:1024\n\t"
                 "global_load_lds_dwordx4 %[va], vcc offset:2048\n\tglobal_load_lds_dwordx4 %[vb], vcc offset:3072\n\t"
                 "s_addk_i32 m0, 0x1000\n\ts_add_u32 vcc_lo, vcc_lo, 0x1000\n\ts_addc_u32 vcc_hi, vcc_hi, 0\n\t"
                 "global_load_lds_dwordx4 %[vc], vcc\n\tglobal_load_lds_dwordx4 %[va], vcc offset:1024\n\t"
                 "global_load_lds_dwordx4 %[vb], vcc offset:2048\n\tglobal_load_lds_dwordx4 %[vc], vcc offset:3072\n\t"
                 "s_waitcnt lgkmcnt(0)\n\ts_branch 1f\n"
                 "2:\n\ts_xor_b32 %[s0], %[b], 24\n\ts_lshr_b32 %[s1], %[s0], 4\n\ts_or_b32 %[s1], %[s1], %[tf]\n\ts_cbranch_scc1 1f\n\t"
                 "s_getreg_b32 %[s2], hwreg(HW_REG_XCC_ID)\n\tv_lshlrev_b32 %[va], 4, %[id]\n\tv_lshl_add_u32 %[va], %[s0], 7, %[va]\n\tv_add_u32 %[vc], 0x2000, %[va]\n\t"
                 "s_and_b32 %[s1], %[s0], 1\n\ts_and_b32 %[s4], %[s0], 8\n\ts_bfe_u32 %[s0], %[s0], 0x20001\n\ts_lshl2_add_u32 %[s0], %[s1], %[s0]\n\ts_or_b32 %[s0], %[s0], %[s4]\n\t"
                 "s_lshl_b32 %[s1], 0x400000, %[s1]\n\t"
                 "s_lshl_b32 %[s0], %[s0], 3\n\tv_mov_b32 %[vb], %[s0]\n\ts_sub_i32 %[s3], %[s2], %[b]\n\ts_and_b32 %[s3], %[s3], 7\n\ts_lshl_b32 %[s3], %[s3], 12\n\t"
                 "s_mov_b64 exec, 0xff\n\ts_waitcnt lgkmcnt(0)\n\t"
                 "s_add_u32 vcc_lo, s16, 0x3fffff\n\ts_addc_u32 vcc_hi, s17, 0\n\ts_and_b32 vcc_lo, vcc_lo, 0xffc00000\n\t"
                 "s_add_u32 vcc_lo, vcc_lo, %[eo]\n\ts_addc_u32 vcc_hi, vcc_hi, 0\n\t"
                 "s_add_u32 s20, s18, %[s1]\n\ts_addc_u32 s21, s19, 0\n\ts_and_b32 s20, s20, 0xffe00000\n\ts_cmp_eq_u32 %[s4], 0\n\ts_cselect_b64 s[20:21], vcc, s[20:21]\n\t"
                 "global_store_dwordx4 %[va], %[vq], s[20:21] offset:256 sc1\n\tglobal_store_dwordx4 %[vc], %[vq], s[20:21] offset:256 sc1\n\t"
                 "s_load_dwordx2 s[26:27], vcc, 0\n\ts_load_dwordx2 s[24:25], vcc, %[s0] offset:128\n\t"
                 "s_waitcnt vmcnt(1)\n\ts_memtime s[20:21]\n\ts_waitcnt vmcnt(0)\n\ts_memtime s[22:23]\n\ts_waitcnt lgkmcnt(0)\n\t"
                 "s_sub_u32 s22, s22, s20\n\ts_min_u32 s22, s22, 255\n\ts_sub_i32 %[s1], 320, s22\n\ts_addk_i32 s22, 192\n\t"
                 "s_bitcmp1_b32 %[s2], 2\n\ts_cselect_b32 s22, %[s1], s22\n\ts_or_b32 s22, s22, %[s3]\n\t"
                 "s_add_u32 s20, s26, %[el]\n\ts_addc_u32 s21, s27, %[eh]\n\ts_and_b32 s20, s20, 0xffff0000\n\ts_or_b32 s20, s20, s22\n\t"
                 "v_mov_b64 %[vz], s[20:21]\n\ts_xor_b64 s[24:25], s[24:25], s[20:21]\n\ts_lshr_b64 s[24:25], s[24:25], 16\n\ts_cselect_b64 exec, 1, 0\n\t"
                 "global_store_dwordx2 %[vb], %[vz], vcc offset:128 sc1\n\ts_mov_b64 exec, -1\n1:"
                 : [ka] "=&{s[12:19]}"(ka), [tf] "=&s"(tf), [t0] "=&{s[20:21]}"(t0), "=&{s[22:23]}"(t2), "=&{s[24:25]}"(t3), [s0] "=&s"(s0), [s1] "=&s"(s1), [s2] "=&s"(s2), [s3] "=&s"(s3), [s4] "=&s"(s4),
                   [va] "=&v"(va), [vb] "=&v"(vb), [vc] "=&v"(vc), [vd] "=&v"(vd), [ve] "=&v"(ve), [vf] "=&v"(vf), [vg] "=&v"(vg), [vq] "=&v"(vq), [vz] "=&v"(vz), [tg] "=&{s[26:27]}"(tag)
                 : [kp] "s"(__builtin_amdgcn_kernarg_segment_ptr()), [id] "v"(threadIdx.x), [b] "s"(b), [sb] "s"(sbase + 4096u), [co] "i"(F_CNT - 4096), [eo] "i"((unsigned)EP_OFF), [el] "i"((unsigned)EP_STEP), [eh] "i"((unsigned)(EP_STEP >> 32)), [fo] "i"((unsigned)CN_OFF - 32768u)
                 : "memory", "scc", "vcc");
  }
  int lane = threadIdx.x & 63;
  const int wave = tf >> 6, w4 = wave & 3;
  int g = lane >> 4, q = (lane >> 2) & 3, p = lane & 3, r;
  const int c = lane & 15;
  char* w_;
  unsigned fo = 0;
#define ARRIVE4(n, sh) do { unsigned n_; \
    asm volatile("s_waitcnt vmcnt(%4)\n\ts_mov_b64 exec, 1\n\tds_add_rtn_u32 %0, %1, %2\n\ts_waitcnt lgkmcnt(0)\n\tv_bfe_u32 %0, %0, %3, 8\n\t" \
                 "v_cmp_eq_u32 vcc, 3, %0\n\ts_nop 4\n\ts_mul_i32 exec_lo, vcc_lo, 0xffff\n\tglobal_store_dwordx2 %5, %6, %7 sc1\n\ts_mov_b64 exec, -1" \
                 : "=&v"(n_) : "v"(sbase + F_CNT), "v"(1u << (sh)), "s"(sh), "i"(n), "v"((threadIdx.x & 63) * 8), "v"(tag), "s"(w_ + CN_OFF + fo) : "memory", "vcc"); } while (0)
  if (__builtin_expect(wave < 4 && b >= F_NW0, 0)) {
    STAMP(0);
    u32x4 tw[2];
    {
      const float fa = (float)((16 * (wave & 1) + c) | 1) * (1.f / 64.f), f0 = __builtin_fmaf(fa, (float)(8 * g), (c & 1) ? 0.25f : 0.f);
#pragma unroll
      for (int s = 0; s < 4; ++s) {
        const float v0 = __builtin_amdgcn_cosf(__builtin_fmaf(fa, (float)(2 * s), f0)), v1 = __builtin_amdgcn_cosf(__builtin_fmaf(fa, (float)(2 * s + 1), f0));
        const unsigned h = pack2(v0, v1);
        tw[0][s] = h;
        tw[1][s] = pack2(v0 - __builtin_bit_cast(float, h << 16), v1 - __builtin_bit_cast(float, h & 0xffff0000u));
      }
    }
    char* ws = w_ = WSP(2);
    const char* ep = ws + EP_OFF;
    SB0();
    const int L = (b & 7) * 28 + ((b - F_NW0) >> 3);
    const int tp = L & 3;
    const int tl = wave >> 1, ct = wave & 1;
    unsigned tb[2][2], ts[2];
#pragma unroll
    for (int hh = 0; hh < 2; ++hh) {
      const unsigned px = 28 * tl + 3 + 8 * g + 4 * hh + q, base = sbase + px * 192 + p * 8, gb = (px & 8) << 2;
      tb[0][hh] = base + gb; tb[1][hh] = base - gb;
      const unsigned ps = 28 * tl + 2 + 32 * (q & 1) + (q >> 1);
      ts[hh] = sbase + g * F_RS + ps * 192 + (((4 * ct + hh) * 32) ^ ((ps & 8) << 2)) + p * 8;
    }
        const u32x4 rs = {(unsigned)(size_t)gXh, (unsigned)((size_t)gXh >> 32) & 0xffffu, NFW * XF, 0x00020000u};
    const unsigned vin = (unsigned)(2 * tp * XT + (L >> 2) * 256) + c * 16;
    const unsigned vo = (unsigned)((8 * ct + 2 * g) * XF + tl * XT) + vin;
    const unsigned long long xw = (unsigned long long)(ws + XW_OFF);
    const u32x4 rs2 = {(unsigned)xw, (unsigned)(xw >> 32) & 0xffffu, NFW * XF2, 0x00020000u};
    const unsigned vi2 = vin + tl * XT;
    unsigned vo2 = (unsigned)(8 * ct + 2 * g) * XF2 + ((vi2 >> 13) << 14) + (vi2 & 8191);
    const unsigned vs = (g < 2) ? g * XC + vi2 : OOB, vs2 = (g < 2) ? g * XC2 + ((vi2 >> 13) << 14) + (vi2 & 8191) : OOB;
    u32x4 sel[2];
#pragma unroll
    for (int i = 0; i < 8; ++i) sel[i >> 2][i & 3] = ((g >> 1) == (i >> 2) && c == 4 * ((i >> 1) & 1) + 2 * (g & 1) + (i & 1)) ? 0x3f803f80u : 0u;
    {
      LANDED(0);
      asm volatile("" : "+v"(tw[0]), "+v"(tw[1]));
      tag += EP_STEP;
    }
    STAMP(1);
    PLD();
    u32x2 da[2][8];
    f32x4 acc[2][4];
    u32x4 o;
#define F_RD(it, k_) TRR(da[(it) & 1][k_], tb[(it) & 1][(k_) & 1], (it) * 32 + ((k_) >> 1) * F_RS)
#define F_EPI(it, m) do { const f32x4 (&e_)[4] = acc[(it) & 1]; const int s_ = ((m) >> 2) * 2; \
      if (((m) & 3) == 1) { o[0] = pack2(e_[0][s_], e_[0][s_ + 1]); o[1] = pack2(e_[1][s_], e_[1][s_ + 1]); } \
      if (((m) & 3) == 2) { o[2] = pack2(e_[2][s_], e_[2][s_ + 1]); o[3] = pack2(e_[3][s_], e_[3][s_ + 1]); } \
      if (((m) & 3) == 3) { if ((it) < 2) BST(o, vo, rs, (it) * XC + ((m) >> 2) * XF); else BST(o, vo2, rs2, ((it) - 2) * XC2 + ((m) >> 2) * XF2); } } while (0)
#pragma unroll
    for (int k = 0; k < 8; ++k) F_RD(0, k);
#pragma unroll
    for (int it = 0; it < 6; ++it) {
      if (it == 1) { STAMP(3); GATE(W_GATE, W_SLOPE, W_OFF); STAMP(2); __builtin_amdgcn_s_setprio(XPR); SB0(); }
      if (it == 3) { STAMP(10); PLC(4); vo2 |= ((((PMASK(pl) ^ 0xaau) >> (4 * ct + g)) & 1u) << 13); STAMP(7); SB0(); }
#pragma unroll
      for (int m = 0; m < 8; ++m) {
        u32x2 (&d_)[8] = da[it & 1];
        if ((m & 1) == 0) WAIT2((it < 5 || m < 4) ? 6 : 10 - m, d_[m], d_[m + 1]);
        f32x4& a_ = acc[it & 1][m >> 1];
        a_ = mfma32(tw[(m & 1) ^ 1], cat(d_[m & 6], d_[m | 1]), (m & 1) ? a_ : Z4);
        SB0();
        if (it < 5) F_RD(it + 1, m);
        else if (m < 4) TRR(da[0][m], ts[m & 1], (m >> 1) * 64);
        if (it > 0) F_EPI(it - 1, m);
        SB0();
      }
    }
    WAIT4(0, da[0][0], da[0][1], da[0][2], da[0][3]);
#pragma unroll
    for (int m = 0; m < 4; ++m) acc[0][m] = mfma32(sel[m & 1], cat(da[0][m & 2], da[0][m | 1]), Z4);
    SB0();
#pragma unroll
    for (int m = 0; m < 8; ++m) F_EPI(5, m);
    if (ct == 0) { BST(pack8(acc[0][0], acc[0][1]), vs, rs, 16 * XF); BST(pack8(acc[0][2], acc[0][3]), vs2, rs2, 16 * XF2); }
    else BST(pack8(acc[0][0], acc[0][1]), vs2, rs2, 16 * XF2 + 2 * XC2);
    STAMP(4);
    __builtin_amdgcn_s_setprio(3);
    fo = (L * 4 + wave) * 128;
    VMW(0); ARRIVEL(ws + CN_OFF + fo); BAR();
  } else {
    __builtin_amdgcn_s_setprio(1);
    unsigned pf[2];
    {
      unsigned cl;
      asm volatile("s_cmp_ge_u32 %4, %5\n\ts_cbranch_scc1 1f\n\t"
                   "s_getpc_b64 %3\nfftconv_code_pc:\n\t"
                   "s_mov_b32 %0, fftconv_code_end-fftconv_code_pc\n\t"
                   "v_lshlrev_b32 %2, 6, %6\n\tv_min_u32 %1, %0, %2\n\tv_min_u32 %2, 0x4fc0, %2\n\t"
                   "global_load_dword %1, %1, %3\n\tglobal_load_dword %2, %2, %7\n1:"
                   : "=&s"(cl), "=&v"(pf[0]), "=&v"(pf[1]), "=&s"(cb)
                   : "s"(b), "i"(F_NPF), "v"(threadIdx.x), "s"(&kTabM) : "memory", "scc");
    }
    const int bj = min(b >> 4, (b >> 5) + 1), cc = (bj * 11) >> 5, wp = bj - 3 * cc, ioc = 4 * cc + (b & 3);
    const int wi = b >= F_NW0 ? b >> 3 : 2 * (b >> 3) + (wave < 4 ? 1 : 0);
    unsigned wv[2];
#pragma unroll
    for (int i = 0; i < 2; ++i) {
      const unsigned hi = lane >> 5, j = ((lane & 31) >> 1) ^ (4 * i + ((2 * w4 + hi) & 3));
      wv[i] = (w4 < 3 ? hi : 0) * (CH * CH * 2) + ((j >> 2) + 8 * (j & 2)) * (CH * 2) + (j & 1) * 32 + (lane & 1) * 16;
    }
    unsigned oW = (unsigned)((32 * (ioc >> 2) + 4 * (ioc & 3)) * (CH * 2) + 64 * wp + 2 * w4 * (CH * CH * 2));
    asm volatile("" : "+v"(wv[0]), "+v"(wv[1]));
    unsigned xcc;
    asm volatile("s_getreg_b32 %0, hwreg(HW_REG_XCC_ID)" : "=s"(xcc));
    unsigned rot = xcc - b;
    asm volatile("" : "+s"(oW));
    KWAIT();
    STAMP(0);
    char* ws = w_ = WSP(2);
    const char* ep = ws + EP_OFF;
    asm volatile("s_cmp_ge_u32 %2, 32\n\ts_cbranch_scc1 1f\n\ts_load_dwordx2 %0, %1, 0\n\ts_barrier\n1:" : "+s"(tag) : "s"(ep), "s"(b) : "memory", "scc");
    if (b < F_NW0) WDMA();
    STAMP(10);
    SB0();
    u32x4 e[2][2], wq[2][4];
    unsigned ta[2][8];
#pragma unroll
    for (int j = 0; j < 8; ++j) {
      ta[0][j] = sbase + F_WOFF + (8 * g + q) * 512 + ((j ^ (4 * (g & 1) + q)) << 5) + p * 8;
      ta[1][j] = ta[0][j] + (g == 3 ? 8192 : 16384);
      asm volatile("" : "+v"(ta[0][j]), "+v"(ta[1][j]));
    }
#define W_UE() do { u = (b - ((b + rot) & 3) + (wi & 3)) & 7; make_e(comp_of(u, 16 * w4 + c), g, e); \
      asm volatile("" : "+v"(e[0][0]), "+v"(e[0][1]), "+v"(e[1][0]), "+v"(e[1][1])); } while (0)
    unsigned vo, vo1;
    int u;
    W_UE();
    SB0();
    STAMP(1);
    if (b >= F_NW0) WDMA();
    LANDED(1);
    asm volatile("" : "+v"(pf[0]), "+v"(pf[1]), "+s"(t0), "+s"(tag));
    tag += EP_STEP;
    PLD();
    STAMP(9);
   for (bool first = true;;) {
    u32x2 d[3][4];
    f32x4 acc[4];
#define W_RD(n, k_) TRR(d[(n) % 3][k_], ta[(k_) >> 1][(2 * (n) + ((n) >> 3)) & 7], (((n) >> 2) & 1) * 256 + ((k_) & 1) * 2048)
#pragma unroll
    for (int k = 0; k < 8; ++k) W_RD(k >> 2, k & 3);
#pragma unroll
    for (int n = 0; n < 16; ++n) {
      u32x2 (&d_)[4] = d[n % 3];
      WAIT4((n < 15) ? 4 : 0, d_[0], d_[1], d_[2], d_[3]);
#pragma unroll
      for (int m = 0; m < 4; ++m) {
        acc[n & 3] = mfma32(e[m & 1][(m >> 1) ^ 1], cat(d_[2 * (m & 1)], d_[2 * (m & 1) + 1]), m ? acc[n & 3] : Z4);
        SB0();
        if (n < 14) W_RD(n + 2, m);
        if (n > 2 && (n & 1)) wq[(n - 3) >> 3][m][((n - 3) >> 1) & 3] = pack2(acc[(n - 3) & 3][m], acc[(n - 2) & 3][m]);
        SB0();
      }
    }
#pragma unroll
    for (int r = 0; r < 4; ++r) wq[1][r][3] = pack2(acc[2][r], acc[3][r]);
    asm volatile("" : "+v"(wq[0][0]), "+v"(wq[0][1]), "+v"(wq[0][2]), "+v"(wq[0][3]), "+v"(wq[1][0]), "+v"(wq[1][1]), "+v"(wq[1][2]), "+v"(wq[1][3]));
    STAMP(3);
    if (!first) break;
    first = false;
    PLC(0);
    if (__builtin_expect(((pl ^ rot) & 3u) == 0, 1)) break;
    rot = pl;
    W_UE();
   }
    {
      const int cp0 = comp_of(u, 16 * w4 + 4 * g), fw = (cp0 * 2341) >> 16;
      vo = (unsigned)(fw * WF + (cp0 - 28 * fw) * (WK / 2) + (ioc & 3) * 256 + c * 16) | ((fw < 16 ? (PMASK(pl) >> u) ^ fw : pl >> 3) & 1u) << 13;
      vo1 = vo ^ 8192u;
    }
    const u32x4 rsw = {(unsigned)(size_t)ws, (unsigned)((size_t)ws >> 32) & 0xffffu, NFW * WF, 0x00020000u};
    unsigned so[4] = {(unsigned)(wp * WP + (ioc >> 2) * 2048)};
    so[1] = so[0] + WK; so[2] = so[0] + WH; so[3] = so[1] + WH;
    unsigned long long ex = ~0ull >> ((w4 + 1) * 8 & 32);
    asm volatile("" : "+s"(so[0]), "+s"(so[1]), "+s"(so[2]), "+s"(so[3]), "+s"(ex));
#define WST(m, v1) asm volatile("s_mov_b64 exec, %0\n\t" \
      "buffer_store_dwordx4 %8, %1, %3, %4 offen sc1\n\tbuffer_store_dwordx4 %9, %1, %3, %4 offen offset:1024 sc1\n\t" \
      "buffer_store_dwordx4 %10, %1, %3, %5 offen sc1\n\tbuffer_store_dwordx4 %11, %1, %3, %5 offen offset:1024 sc1\n\t" \
      "buffer_store_dwordx4 %12, %2, %3, %6 offen sc1\n\tbuffer_store_dwordx4 %13, %2, %3, %6 offen offset:1024 sc1\n\t" \
      "buffer_store_dwordx4 %14, %2, %3, %7 offen sc1\n\tbuffer_store_dwordx4 %15, %2, %3, %7 offen offset:1024 sc1\n\ts_mov_b64 exec, -1\n\ts_nop 0" \
      :: "s"(m), "v"(vo), "v"(v1), "s"(rsw), "s"(so[0]), "s"(so[1]), "s"(so[2]), "s"(so[3]), \
         "v"(wq[0][0]), "v"(wq[0][1]), "v"(wq[0][2]), "v"(wq[0][3]), "v"(wq[1][0]), "v"(wq[1][1]), "v"(wq[1][2]), "v"(wq[1][3]) : "memory")
    asm volatile("" : "+v"(vo), "+v"(vo1));
    STAMP(7);
    GATE(WG_W, 64, 0);
    STAMP(2);
    const int item = u * 36 + 4 * bj + (b & 3);
    if (w4 == 3) {
      WST(0xffffull << 32, vo1);
      if (b >= F_NW0) { VMW(0); ARRIVE(ws + CN_OFF + FL_V + item * 8); }
    }
    if (HOLD) BAR();
    STAMP(13);
    WST(ex, vo);
    STAMP(4);
    if (!HOLD) BAR();
    if (w4 == 3 && b < F_NW0) { VMW(8); ARRIVE(ws + CN_OFF + FL_V + item * 8); }
    fo = FL_W + item * 128;
    ARRIVE4(0, 16 + 2 * (wave & 4));
  }

  pl = __builtin_amdgcn_readfirstlane(pl);
  unsigned pm = PMASK(pl), cbit, st;
  u32x2 pv;
  char* outp = KPTR(3);
  asm volatile("" : "+s"(pm), "+s"(outp), "+s"(w_), "+s"(tag));
  const int slot = b & 7, lj = b >> 3;
  const int L = b >= F_NW0 ? 28 * slot + lj - 4 : 224 + 8 * (lj ^ 2) + 2 * ((b + pl) & 3) + (((b + pl) >> 2) & 1);
  const int fw = L / 14, G = (L - 14 * fw) >> 1, half = L & 1;
  int Pc = max(28 * (int)((b + pl) & 3) + lj - 4, 0);
  unsigned co = ((unsigned)(size_t)outp & 0x1fffffu) | ((((pl >> 3) & 1u) * 3u) ^ (pl >> 4)) << 30;
  asm volatile("" : "+s"(Pc), "+s"(co));
  unsigned vL = lane * 16;
  const char* tabp = (const char*)&kTabM;
  u32x4 fh[2][2], fi[2], gi[2][2];
  const int Q = 8 * G + wave;
  unsigned oX = (unsigned)(fw * XF + Q * 2048);
  int k0, k1, mw;
  unsigned xl0;
  const char *xs = gXh + oX, *xsh, *xs2, *xsh2, *ws[2];
  unsigned zw_, yv[2], fia;
  int px0;
  {
    const int Qz = Q + ((lane >> 3) & 4), t = (Qz * 37) >> 8;
    int i = 8 * (Qz - 7 * t) + min((lane & 31) >> 1, 11);
    i -= i >= 56 ? 56 : 0;
    const unsigned z = (unsigned)((i * 4 + (t >> 1)) * 4 + 2 * (t & 1) + (fw < 16 ? fw >> 3 : lane & 1)) * 128;
    zw_ = fw < 16 ? FL_W + (slot * 36 + min(wave * 5 + min(lane, 4), 35)) * 128 : FL_V + min(wave * 32 + (lane & 31), 251) * 8;
    r = lane & 15;
#pragma unroll
    for (int s_ = 0; s_ < 2; ++s_)
      yv[s_] = ((KTIL(Q, 2 * s_ + (g & 1)) ^ (r >> 3) ^ (Q >> 1)) & 1u) * 8192 + (((4 * (g & 1) + ((r >> 1) & 3)) ^ (fw & 3)) << 6) + (r & 1) * 32 + (g >> 1) * 16;
    fia = sbase + M_TAB + (fw == 16 ? 2048 : 0) + (vL ^ (KROW(Q, (r >> 1) & 3) << 7));
    const int tq = ((Pc >> 1) * 37) >> 8, row0 = 16 * (Pc & 1) + 2 * (wave & 4) + (wave & 3);
    px0 = (32 * ((Pc >> 1) - 7 * tq) + row0 + 4 * ((KROW(Pc >> 1, wave & 3) ^ KTIL(Pc >> 1, row0 >> 3) ^ (pm >> slot)) & 1u)) * IMG + 28 * tq;
    unsigned z1 = z;
    fo = __builtin_amdgcn_readfirstlane(fo);
    asm volatile("" : "+v"(yv[0]), "+v"(yv[1]), "+v"(fia), "+v"(z1), "+v"(zw_));
    asm volatile("" : "+s"(px0), "+s"(xs), "+s"(fo));
    STAMP(5);
  if (__builtin_expect(L >= M_NI, 0)) {
    if (L == M_NI) {
      asm volatile("s_sleep 127\n\ts_sleep 127" ::: "memory");
#pragma unroll
      for (int i = 0; i < 3; ++i) {
        const unsigned z = min(threadIdx.x + 512u * i, 1183u) * 128u;
        POLLA(z, w_ + CN_OFF, 7); CHECKA(0, z, w_ + CN_OFF, 7, 8);
      }
      BAR();
      if (wave == 0) ARRIVE(w_ + EP_OFF);
    }
    STAMP_MID(); STAMP_END(10);
    return;
  }
    STAMP(14);
    if (wave < 4) {
      POLLA(z1, w_ + CN_OFF, 7); CHECKA(0, z1, w_ + CN_OFF, 7, F_SLEEP);
      asm volatile("ds_write_b8 %0, %1 offset:4" :: "v"(sbase + F_CNT + wave), "v"(1u) : "memory");
    } else {
      unsigned n_;
      asm volatile("1:\n\tds_read_u8 %0, %1\n\ts_waitcnt lgkmcnt(0)\n\tv_cmp_eq_u32 vcc, 0, %0\n\ts_nop 4\n\ts_cbranch_vccz 2f\n\ts_sleep 1\n\ts_branch 1b\n2:"
                   : "=&v"(n_) : "v"(sbase + F_CNT + wave) : "memory", "vcc");
    }
    STAMP(15);
  }
  STAMP_MID();
  STAMP(0);
#define POLLW() POLLA(zw_, w_ + CN_OFF, 7)
#define CHECKW(n) CHECKA(n, zw_, w_ + CN_OFF, 7, F_SLEEP)
#define XPIN() asm volatile("" : "+s"(xs))
  char *ys, *Yh;
  char *cnt, *flp;
  u32x4 xr[3][2][2];
  u32x2 xh[3][2];
#define TABF() do { int l_ = L; asm volatile("" : "+s"(l_)); const char* tf = tabp + (l_ >= 224 ? 4096 : 0); xsh = xs + (Q - 7 * ((Q * 37) >> 8) == 6 ? 2048 - XT : 2048); \
    GLD(fh[0][0], vL, tf, 0); GLD(fh[1][0], vL, tf, 2048); if (H_LO) { GLD(fh[0][1], vL, tf, 1024); GLD(fh[1][1], vL, tf, 3072); } } while (0)
#define SETUP_X() do { \
    const int qh_ = Q - 7 * ((Q * 37) >> 8) == 6 ? Q - 6 : Q + 1; \
    unsigned o_ = (unsigned)XW_OFF + fw * XF2 + ((fw >> 1) & 1) * 8192; \
    unsigned o1_ = o_ + (Q >> 2) * 16384 + (Q & 3) * 2048, o2_ = o_ + (qh_ >> 2) * 16384 + (qh_ & 3) * 2048; \
    asm volatile("" : "+s"(o1_), "+s"(o2_)); \
    cbit = ((pm >> slot) & 1u) << 13; \
    xs2 = w_ + (o1_ ^ (fw < 16 ? cbit : 0u)); xsh2 = w_ + (o2_ ^ (fw < 16 ? cbit : 0u)); \
    asm volatile("" : "+s"(w_), "+s"(xs2), "+s"(xsh2)); \
  } while (0)
#define SETUP_P() do { \
    int w8_ = wave; asm volatile("" : "+s"(w8_)); \
    k0 = w8_ < 4 ? 2 * w8_ : 4 + w8_; k1 = w8_ < 4 ? k0 + 1 : 10 + (w8_ >> 1); mw = w8_ < 4 ? 0 : w8_ & 1; \
    xl0 = sbase + w8_ * (4 * M_XT); \
    ws[0] = w_ + (WT_OFF + (((fw * 2 + half) * WH + k0 * WK + (fw & 1) * 8192 + 2048) ^ cbit)); ws[1] = ws[0] + (k1 - k0) * WK; \
    Yh = w_ + YH_OFF; cnt = w_ + CN_OFF + FL_Y; \
    ys = Yh + (Q * YQ + (fw >> 3) * 16384 + (fw & 7) * 1024 + half * 512); \
    flp = cnt + (G * 8 + wave) * CN_G + (fw * 2 + half) * 8;         \
    asm volatile("" : "+v"(vL)); \
    lane = vL >> 4; g = lane >> 4; r = lane & 15; q = (lane >> 2) & 3; p = lane & 3; \
  } while (0)

  unsigned zr[2], to[4];
  u32x10 dv[2];
  f32x4 tacc[4][2][2];
#define SETUP_Z() do { \
    zr[0] = sbase + M_RB + wave * (4 * M_ZT) + (2 * g + (q >> 1)) * 64 + p * 16 + (q & 1) * 8; zr[1] = zr[0] + (g == 3 ? 384 : 512); \
  } while (0)
#define TDV(cc) do { _Pragma("unroll") for (int c_ = 0; c_ < 2; ++c_) { const u32x4 a_ = xr[cc][c_][0], b_ = xr[cc][c_][1]; \
      dv[c_] = u32x10{a_[0], a_[1], a_[2], a_[3], b_[0], b_[1], b_[2], b_[3], xh[cc][c_][0], xh[cc][c_][1]}; } \
    asm volatile("" : "+v"(dv[0]), "+v"(dv[1])); } while (0)
#define TMF(htl, chg, ct) do { const u32x4 b_ = __builtin_shufflevector(dv[chg], dv[chg], 2 * (htl), 2 * (htl) + 1, 2 * (htl) + 2, 2 * (htl) + 3); \
    MM(tacc[htl][ct][chg], fh[ct][0], b_, H_LO ? mfma32(fh[ct][1], b_, Z4) : Z4); } while (0)
#define TCV(i, htl, e) do { to[i] = pack2(tacc[htl][(e) >> 2][0][(e) & 3], tacc[htl][(e) >> 2][1][(e) & 3]); \
    if ((e) == 6) asm volatile("" :: "v"(to[i]), "v"(tacc[htl][1][0]), "v"(tacc[htl][1][1])); } while (0)
#define TM0(cc) asm volatile("s_mov_b32 m0, %0\n\ts_nop 0" :: "s"(xl0 + ((cc) == 1 ? 8192 : 0)) : "memory")
#define TWR(i, cc, htl, e) asm volatile("ds_write_addtid_b32 %0 offset:%1" :: "v"(to[i]), "i"(((cc) == 1 ? M_RB - 8192 : 0) + (htl) * M_XT + (e) * 256) : "memory")
#define TABD() DMA(sbase + M_TAB + wave * 1024, vL, tabp + 8192 + wave * 1024, 0)
#define TABI() do { DSR(fi[0], fia, 0); if (H_LO) DSR(fi[1], fia, 1024); } while (0)
#define TABG() do { const unsigned a_ = sbase + M_TAB + vL; DSR(gi[0][0], a_, 4096); DSR(gi[0][1], a_, 5120); DSR(gi[1][0], a_, 6144); DSR(gi[1][1], a_, 7168); } while (0)
  u32x2 zd[2][4];
#define ZRD(s, zo) do { TRR(zd[s][0], zr[0], zo); TRR(zd[s][1], zr[1], zo); TRR(zd[s][2], zr[0], (zo) + M_ZT); TRR(zd[s][3], zr[1], (zo) + M_ZT); } while (0)
  f32x4 zy[2][2];
  u32x4 zo;
#define ZIW(s, n) WAIT4(n, zd[s][0], zd[s][1], zd[s][2], zd[s][3])
#define ZIM(s, t) do { const u32x4 a_ = cat(zd[s][2 * (t)], zd[s][2 * (t) + 1]); MM(zy[s][t], a_, fi[0], H_LO ? mfma32(a_, fi[1], Z4) : Z4); } while (0)
  unsigned zc[4];
#define ZIC(s, i) do { if ((i) < 2) { zc[2 * (i)] = pack2(zy[s][0][2 * (i)], zy[s][0][2 * (i) + 1]); zc[2 * (i) + 1] = pack2(zy[s][1][2 * (i)], zy[s][1][2 * (i) + 1]); \
      PIN(zc[2 * (i)]); PIN(zc[2 * (i) + 1]); } \
    else { const auto s0_ = __builtin_amdgcn_permlane16_swap(zc[0], zc[1], false, false), s1_ = __builtin_amdgcn_permlane16_swap(zc[2], zc[3], false, false); \
      zo = u32x4{s0_[0], s1_[0], s0_[1], s1_[1]}; PIN(zo); } } while (0)
#define ZINV2(p_) do { ZIW(0, 4); ZIM(0, 0); ZIM(0, 1); ZIW(1, 0); ZIM(1, 0); ZIM(1, 1); SB0(); \
    ZIC(0, 0); ZIC(0, 1); ZIC(0, 2); YST(zo, ys + (p_) * YG, 0); ZIC(1, 0); ZIC(1, 1); ZIC(1, 2); YST(zo, ys + (p_) * YG + 2 * YT, 1); } while (0)
#define flc (cnt + (Pc >> 1) * CN_G)
#define src (Yh + (((Pc >> 1) * YQ + (Pc & 1) * (2 * YT) + ((Pc >> 2) & 1) * 8192) ^ cbit))
#define ARRA(p) asm volatile("s_mov_b64 exec, 1\n\tglobal_atomic_swap_x2 %0, %1, %2 sc1\n\ts_mov_b64 exec, -1" :: "v"(0u), "v"(tag), "s"(p) : "memory")
#define ARRY(s_) do { if (s_) ARRA(flp + (s_) * CN_P); else ARRIVE(flp); } while (0)
#define POLLT(z_, p_, w_) asm volatile("s_cmp_lg_u32 %3, 7\n\ts_cbranch_scc1 2f\n\ts_mov_b32 exec_hi, 3\n\tglobal_atomic_or_x2 %0, %1, %4, %2 sc0 sc1\n\ts_mov_b32 exec_hi, -1\n2:" \
    : "=&v"(pv) : "v"(z_), "s"(p_), "s"(w_), "v"(0ull) : "memory", "scc")
#define CHECKT(n, z_, p_, w_, sl_) asm volatile("s_mov_b32 %[sp], 0\n\ts_waitcnt vmcnt(%[nn])\n\ts_cmp_lg_u32 %[wv], 7\n\ts_cbranch_scc1 2f\n\ts_mov_b32 exec_hi, 3\n" \
    "1:\n\tv_cmp_ne_u64 vcc, %[tg], %[v]\n\ts_nop 4\n\ts_cbranch_vccz 2f\n\t" \
    "s_add_u32 %[sp], %[sp], 1\n\ts_cmp_ge_u32 %[sp], %[lim]\n\ts_cbranch_scc1 2f\n\ts_mov_b64 exec, vcc\n\ts_sleep %[sl]\n\t" \
    "global_atomic_or_x2 %[v], %[z], %[zero], %[p] sc0 sc1\n\ts_waitcnt vmcnt(0)\n\ts_branch 1b\n2:\n\ts_mov_b64 exec, -1" \
    : [v] "+v"(pv), [sp] "=&s"(st) : [z] "v"(z_), [p] "s"(p_), [tg] "s"(tag), [lim] "i"(SPIN_MAX), [nn] "i"(n), [sl] "i"(sl_), [wv] "s"(w_), [zero] "v"(0ull) : "memory", "scc", "vcc")
#define POLL(s_) POLLT(min(vL >> 1, 264u), flc + (s_) * CN_P, wave)
#define CHECK(n, s_) CHECKT(n, min(vL >> 1, 264u), flc + (s_) * CN_P, wave, POLL_SLEEP)

#define srw (src + wave * 1024)
#define il (sbase + M_RA + wave * 1024)
#define IDMA(s_) do { asm volatile("s_cmp_lg_u32 %3, 0\n\ts_cbranch_scc1 1f\n\ts_mov_b32 m0, %0\n\ts_nop 0\n\tglobal_load_lds_dwordx4 %1, %2 sc1\n1:" \
      :: "s"(il + (s_) * YS + 16384), "v"(vL), "s"(srw + (s_) * YG + 32768), "s"(wave) : "memory", "scc"); \
    DMA1(il + (s_) * YS, vL, srw + (s_) * YG, 0); DMA1(il + (s_) * YS + 8192, vL, srw + (s_) * YG + 16384, 0); } while (0)
  {
    u32x4 wf[36], xt[3][2][2][2];
    f32x4 aP[3][2][2], aQ[3][2][2], aI[3][2][2];
    unsigned xa[2], xb[2], za[2];
#define SETUP_M() do { \
      xa[0] = sbase + r * M_XT + g * 16 + k0 * 128; xa[1] = xa[0] + (k1 - k0) * 128 + mw * (16 * M_XT); xb[0] = xa[0] + M_RB; xb[1] = xa[1] + M_RB; \
      za[0] = sbase + M_RB + r * M_ZT + g * 16 + k0 * 64; za[1] = za[0] + (k1 - k0) * 64 + mw * (16 * M_ZT); \
    } while (0)
#define XRD(cc, kk, ri, mt) DSR(xt[cc][kk][ri][mt], ((cc) == 1 ? xb : xa)[kk], (ri) * 64 + (mt) * (16 * M_XT))
    u32x4 zw;
    float zs[4];
#define ZC(p_, kk, mt, i) do { if ((i) < 2) { zs[2 * (i)] = aP[p_][kk][mt][2 * (i)] - aQ[p_][kk][mt][2 * (i)]; asm volatile("" : "+v"(zs[2 * (i)])); \
      zs[2 * (i) + 1] = aP[p_][kk][mt][2 * (i) + 1] - aQ[p_][kk][mt][2 * (i) + 1]; PIN(zs[2 * (i) + 1]); } \
    else if ((i) == 2) { zw[2] = pack2(aI[p_][kk][mt][0], aI[p_][kk][mt][1]); zw[3] = pack2(aI[p_][kk][mt][2], aI[p_][kk][mt][3]); PIN(zw[2]); PIN(zw[3]); } \
    else { zw[0] = pack2(zs[0], zs[1]); zw[1] = pack2(zs[2], zs[3]); } } while (0)
#define ZW(p_, kk, mt) DSW(za[kk] + ((p_) == 2 ? M_Z1 : 0), zw, ((p_) ? M_Z1 : 0) + (mt) * (16 * M_ZT))
    if (wave < 4) {
    XPIN();
    __builtin_amdgcn_s_setprio(3);
    XLD(0, 0, 0); SB0();
    XLD(0, 1, 0); SB0();
    TABF();
    XLD(0, 0, 1); SB0();
    XLD(0, 1, 1); SB0();
    XLD(0, 0, 2); SB0();
    XLD(0, 1, 2); SB0();
    __builtin_amdgcn_s_setprio(2);
    SETUP_X(); SB0();
    XLD(1, 0, 0); SB0();
    XLD(1, 1, 0); SB0();
    XLD(1, 0, 1); SB0();
    XLD(1, 1, 1); SB0();
    XLD(1, 0, 2); SB0();
    XLD(1, 1, 2); SB0();
    __builtin_amdgcn_s_setprio(0);
    SB0(); SETUP_P(); SETUP_M(); SETUP_Z(); SB0();
    VMW(6); SB0();
    asm volatile("" : XC_(fh[0][0]), XC_(fh[1][0])); SB0();
    STAMP(1);
    TDV(0); SB0();
    TMF(0, 0, 0); SB0();
    TMF(0, 1, 0); SB0();
    XLD(2, 0, 0); SB0();
    TMF(0, 0, 1); SB0();
    TMF(0, 1, 1); SB0();
    XLD(2, 1, 0); SB0();
    TMF(1, 0, 0); SB0();
    TCV(0, 0, 0); SB0();
    TCV(1, 0, 1); SB0();
    TM0(0);
    TWR(0, 0, 0, 0); SB0();
    TCV(2, 0, 2); SB0();
    TMF(1, 1, 0); SB0();
    XLD(2, 0, 1); SB0();
    TWR(1, 0, 0, 1); SB0();
    TCV(3, 0, 3); SB0();
    TWR(2, 0, 0, 2); SB0();
    TMF(1, 0, 1); SB0();
    TCV(0, 0, 4); SB0();
    TWR(3, 0, 0, 3); SB0();
    TCV(1, 0, 5); SB0();
    TWR(0, 0, 0, 4); SB0();
    TMF(1, 1, 1); SB0();
    XLD(2, 1, 1); SB0();
    TCV(2, 0, 6); SB0();
    TWR(1, 0, 0, 5); SB0();
    TWR(2, 0, 0, 6); SB0();
    TMF(2, 0, 0); SB0();
    TCV(3, 1, 0); SB0();
    TCV(0, 1, 1); SB0();
    TWR(3, 0, 1, 0); SB0();
    TCV(1, 1, 2); SB0();
    TMF(2, 1, 0); SB0();
    XLD(2, 0, 2); SB0();
    TWR(0, 0, 1, 1); SB0();
    TCV(2, 1, 3); SB0();
    TWR(1, 0, 1, 2); SB0();
    TMF(2, 0, 1); SB0();
    TCV(3, 1, 4); SB0();
    TWR(2, 0, 1, 3); SB0();
    TCV(0, 1, 5); SB0();
    TWR(3, 0, 1, 4); SB0();
    TMF(2, 1, 1); SB0();
    XLD(2, 1, 2); SB0();
    TCV(1, 1, 6); SB0();
    TWR(0, 0, 1, 5); SB0();
    TWR(1, 0, 1, 6); SB0();
    POLLW(); SB0();
    TMF(3, 0, 0); SB0();
    TCV(2, 2, 0); SB0();
    TCV(3, 2, 1); SB0();
    TWR(2, 0, 2, 0); SB0();
    TCV(0, 2, 2); SB0();
    TMF(3, 1, 0); SB0();
    TWR(3, 0, 2, 1); SB0();
    TCV(1, 2, 3); SB0();
    TWR(0, 0, 2, 2); SB0();
    TMF(3, 0, 1); SB0();
    TABD(); SB0();
    TCV(2, 2, 4); SB0();
    TM0(0);
    TWR(1, 0, 2, 3); SB0();
    TCV(3, 2, 5); SB0();
    TMF(3, 1, 1); SB0();
    TWR(2, 0, 2, 4); SB0();
    TCV(0, 2, 6); SB0();
    TWR(3, 0, 2, 5); SB0();
    TWR(0, 0, 2, 6); SB0();
    VMW(8); SB0();
    TDV(1); SB0();
    TMF(0, 0, 0); SB0();
    TCV(1, 3, 0); SB0();
    TCV(2, 3, 1); SB0();
    TWR(1, 0, 3, 0); SB0();
    TCV(3, 3, 2); SB0();
    TMF(0, 1, 0); SB0();
    TWR(2, 0, 3, 1); SB0();
    TCV(0, 3, 3); SB0();
    TWR(3, 0, 3, 2); SB0();
    TMF(0, 0, 1); SB0();
    TCV(1, 3, 4); SB0();
    TWR(0, 0, 3, 3); SB0();
    TCV(2, 3, 5); SB0();
    TWR(1, 0, 3, 4); SB0();
    TMF(0, 1, 1); SB0();
    TCV(3, 3, 6); SB0();
    TWR(2, 0, 3, 5); SB0();
    TWR(3, 0, 3, 6); SB0();
    TMF(1, 0, 0); SB0();
    TCV(0, 0, 0); SB0();
    TCV(1, 0, 1); SB0();
    TM0(1);
    TWR(0, 1, 0, 0); SB0();
    TCV(2, 0, 2); SB0();
    TMF(1, 1, 0); SB0();
    TWR(1, 1, 0, 1); SB0();
    TCV(3, 0, 3); SB0();
    TWR(2, 1, 0, 2); SB0();
    TMF(1, 0, 1); SB0();
    TCV(0, 0, 4); SB0();
    TWR(3, 1, 0, 3); SB0();
    TCV(1, 0, 5); SB0();
    TWR(0, 1, 0, 4); SB0();
    TMF(1, 1, 1); SB0();
    TCV(2, 0, 6); SB0();
    TWR(1, 1, 0, 5); SB0();
    TWR(2, 1, 0, 6); SB0();
    CHECKW(1); SB0();
    asm volatile("s_waitcnt lgkmcnt(7)" ::: "memory");
    BAR(); SB0();
    STAMP(2);
    WLD(wf[0], vL, ws[0] + 0 * WP, -2048); SB0();
    WLD(wf[1], vL, ws[0] + 0 * WP, -1024); SB0();
    WLD(wf[2], vL, ws[1] + 0 * WP, -2048); SB0();
    WLD(wf[3], vL, ws[1] + 0 * WP, -1024); SB0();
    XRD(0, 0, 0, 0);
    XRD(0, 0, 1, 0);
    XRD(0, 0, 0, 1);
    XRD(0, 0, 1, 1);
    XRD(0, 1, 0, 0);
    XRD(0, 1, 1, 0);
    XRD(0, 1, 0, 1);
    XRD(0, 1, 1, 1);
    SB0();
    TMF(2, 0, 0); SB0();
    WLD(wf[4], vL, ws[0] + 0 * WP, 0); SB0();
    TCV(3, 1, 0); SB0();
    TCV(0, 1, 1); SB0();
    TWR(3, 1, 1, 0); SB0();
    TMF(2, 1, 0); SB0();
    TCV(1, 1, 2); SB0();
    TWR(0, 1, 1, 1); SB0();
    TCV(2, 1, 3); SB0();
    TWR(1, 1, 1, 2); SB0();
    TMF(2, 0, 1); SB0();
    TCV(3, 1, 4); SB0();
    TWR(2, 1, 1, 3); SB0();
    TCV(0, 1, 5); SB0();
    TWR(3, 1, 1, 4); SB0();
    TMF(2, 1, 1); SB0();
    WLD(wf[5], vL, ws[0] + 0 * WP, 1024); SB0();
    TCV(1, 1, 6); SB0();
    TWR(0, 1, 1, 5); SB0();
    TWR(1, 1, 1, 6); SB0();
    TMF(3, 0, 0); SB0();
    TCV(2, 2, 0); SB0();
    TCV(3, 2, 1); SB0();
    TWR(2, 1, 2, 0); SB0();
    TCV(0, 2, 2); SB0();
    TMF(3, 1, 0); SB0();
    TWR(3, 1, 2, 1); SB0();
    TCV(1, 2, 3); SB0();
    TWR(0, 1, 2, 2); SB0();
    TMF(3, 0, 1); SB0();
    WLD(wf[6], vL, ws[1] + 0 * WP, 0); SB0();
    TCV(2, 2, 4); SB0();
    TWR(1, 1, 2, 3); SB0();
    TCV(3, 2, 5); SB0();
    TMF(3, 1, 1); SB0();
    TWR(2, 1, 2, 4); SB0();
    TCV(0, 2, 6); SB0();
    TWR(3, 1, 2, 5); SB0();
    TWR(0, 1, 2, 6); SB0();
    TCV(1, 3, 0); SB0();
    TCV(2, 3, 1); SB0();
    TWR(1, 1, 3, 0); SB0();
    TCV(3, 3, 2); SB0();
    TWR(2, 1, 3, 1); SB0();
    TCV(0, 3, 3); SB0();
    TWR(3, 1, 3, 2); SB0();
    TCV(1, 3, 4); SB0();
    TWR(0, 1, 3, 3); SB0();
    TCV(2, 3, 5); SB0();
    TWR(1, 1, 3, 4); SB0();
    TCV(3, 3, 6); SB0();
    TWR(2, 1, 3, 5); SB0();
    TWR(3, 1, 3, 6); SB0();
    asm volatile("s_waitcnt lgkmcnt(0)" ::: "memory");
    BAR(); SB0();
    STAMP(3);
    XRD(1, 0, 0, 0);
    XRD(1, 0, 1, 0);
    XRD(1, 0, 0, 1);
    XRD(1, 0, 1, 1);
    XRD(1, 1, 0, 0);
    XRD(1, 1, 1, 0);
    XRD(1, 1, 0, 1);
    XRD(1, 1, 1, 1);
    SB0();
    VMW(9); SB0();
    TDV(2); SB0();
    TMF(0, 0, 0); SB0();
    TMF(0, 1, 0); SB0();
    WLD(wf[7], vL, ws[1] + 0 * WP, 1024); SB0();
    TMF(0, 0, 1); SB0();
    TMF(0, 1, 1); SB0();
    WAITV(6, wf[0], wf[1]); SB0();
    asm volatile("" : XC_(xt[0][0][0][0]), XC_(xt[0][0][1][0])); SB0();
    MM(aP[0][0][0], wf[0], xt[0][0][0][0], Z4); SB0();
    WLD(wf[8], vL, ws[0] + 1 * WP, -2048); SB0();
    TCV(0, 0, 0); SB0();
    TCV(1, 0, 1); SB0();
    MM(aQ[0][0][0], wf[1], xt[0][0][1][0], Z4); SB0();
    TM0(2);
    TWR(0, 2, 0, 0); SB0();
    TCV(2, 0, 2); SB0();
    TWR(1, 2, 0, 1); SB0();
    MM(aI[0][0][0], wf[0], xt[0][0][1][0], Z4); SB0();
    TCV(3, 0, 3); SB0();
    TWR(2, 2, 0, 2); SB0();
    TCV(0, 0, 4); SB0();
    MM(aI[0][0][0], wf[1], xt[0][0][0][0], aI[0][0][0]); SB0();
    WLD(wf[9], vL, ws[0] + 1 * WP, -1024); SB0();
    TWR(3, 2, 0, 3); SB0();
    TCV(1, 0, 5); SB0();
    TMF(1, 0, 0); SB0();
    TWR(0, 2, 0, 4); SB0();
    TCV(2, 0, 6); SB0();
    TWR(1, 2, 0, 5); SB0();
    TMF(1, 1, 0); SB0();
    TWR(2, 2, 0, 6); SB0();
    TMF(1, 0, 1); SB0();
    WLD(wf[10], vL, ws[0] + 0 * WP, 2048); SB0();
    TMF(1, 1, 1); SB0();
    asm volatile("" : XC_(xt[0][0][0][1]), XC_(xt[0][0][1][1])); SB0();
    MM(aP[0][0][1], wf[0], xt[0][0][0][1], Z4); SB0();
    TCV(3, 1, 0); SB0();
    TCV(0, 1, 1); SB0();
    TWR(3, 2, 1, 0); SB0();
    MM(aQ[0][0][1], wf[1], xt[0][0][1][1], Z4); SB0();
    WLD(wf[11], vL, ws[0] + 0 * WP, 3072); SB0();
    TCV(1, 1, 2); SB0();
    TWR(0, 2, 1, 1); SB0();
    MM(aI[0][0][1], wf[0], xt[0][0][1][1], Z4); SB0();
    TCV(2, 1, 3); SB0();
    TWR(1, 2, 1, 2); SB0();
    TCV(3, 1, 4); SB0();
    MM(aI[0][0][1], wf[1], xt[0][0][0][1], aI[0][0][1]); SB0();
    TWR(2, 2, 1, 3); SB0();
    TCV(0, 1, 5); SB0();
    TWR(3, 2, 1, 4); SB0();
    TMF(2, 0, 0); SB0();
    WLD(wf[12], vL, ws[1] + 0 * WP, 2048); SB0();
    TCV(1, 1, 6); SB0();
    TWR(0, 2, 1, 5); SB0();
    TMF(2, 1, 0); SB0();
    TWR(1, 2, 1, 6); SB0();
    TMF(2, 0, 1); SB0();
    TMF(2, 1, 1); SB0();
    WLD(wf[13], vL, ws[1] + 0 * WP, 3072); SB0();
    WAITV(10, wf[2], wf[3]); SB0();
    asm volatile("" : XC_(xt[0][1][0][0]), XC_(xt[0][1][1][0])); SB0();
    MM(aP[0][1][0], wf[2], xt[0][1][0][0], Z4); SB0();
    TCV(2, 2, 0); SB0();
    TCV(3, 2, 1); SB0();
    TWR(2, 2, 2, 0); SB0();
    MM(aQ[0][1][0], wf[3], xt[0][1][1][0], Z4); SB0();
    TCV(0, 2, 2); SB0();
    TWR(3, 2, 2, 1); SB0();
    TCV(1, 2, 3); SB0();
    MM(aI[0][1][0], wf[2], xt[0][1][1][0], Z4); SB0();
    WLD(wf[14], vL, ws[0] + 1 * WP, 0); SB0();
    TWR(0, 2, 2, 2); SB0();
    TCV(2, 2, 4); SB0();
    MM(aI[0][1][0], wf[3], xt[0][1][0][0], aI[0][1][0]); SB0();
    TWR(1, 2, 2, 3); SB0();
    TCV(3, 2, 5); SB0();
    TWR(2, 2, 2, 4); SB0();
    TMF(3, 0, 0); SB0();
    TCV(0, 2, 6); SB0();
    TWR(3, 2, 2, 5); SB0();
    TWR(0, 2, 2, 6); SB0();
    TMF(3, 1, 0); SB0();
    WLD(wf[15], vL, ws[0] + 1 * WP, 1024); SB0();
    TMF(3, 0, 1); SB0();
    TMF(3, 1, 1); SB0();
    asm volatile("" : XC_(xt[0][1][0][1]), XC_(xt[0][1][1][1])); SB0();
    MM(aP[0][1][1], wf[2], xt[0][1][0][1], Z4); SB0();
    WLD(wf[16], vL, ws[0] + 1 * WP, 2048); SB0();
    TCV(1, 3, 0); SB0();
    TCV(2, 3, 1); SB0();
    MM(aQ[0][1][1], wf[3], xt[0][1][1][1], Z4); SB0();
    TWR(1, 2, 3, 0); SB0();
    TCV(3, 3, 2); SB0();
    TWR(2, 2, 3, 1); SB0();
    MM(aI[0][1][1], wf[2], xt[0][1][1][1], Z4); SB0();
    TCV(0, 3, 3); SB0();
    TWR(3, 2, 3, 2); SB0();
    TCV(1, 3, 4); SB0();
    MM(aI[0][1][1], wf[3], xt[0][1][0][1], aI[0][1][1]); SB0();
    WLD(wf[17], vL, ws[0] + 1 * WP, 3072); SB0();
    TWR(0, 2, 3, 3); SB0();
    TCV(2, 3, 5); SB0();
    WAITV(12, wf[4], wf[5]); SB0();
    asm volatile("s_waitcnt lgkmcnt(15)" : XC_(xt[1][0][0][0]), XC_(xt[1][0][1][0]) :: "memory"); SB0();
    MM(aP[0][0][0], wf[4], xt[1][0][0][0], aP[0][0][0]); SB0();
    TWR(1, 2, 3, 4); SB0();
    TCV(3, 3, 6); SB0();
    TWR(2, 2, 3, 5); SB0();
    MM(aQ[0][0][0], wf[5], xt[1][0][1][0], aQ[0][0][0]); SB0();
    TWR(3, 2, 3, 6); SB0();
    MM(aI[0][0][0], wf[4], xt[1][0][1][0], aI[0][0][0]); SB0();
    WLD(wf[18], vL, ws[1] + 1 * WP, -2048); SB0();
    MM(aI[0][0][0], wf[5], xt[1][0][0][0], aI[0][0][0]); SB0();
    asm volatile("" : XC_(xt[1][0][0][1]), XC_(xt[1][0][1][1])); SB0();
    MM(aP[0][0][1], wf[4], xt[1][0][0][1], aP[0][0][1]); SB0();
    MM(aQ[0][0][1], wf[5], xt[1][0][1][1], aQ[0][0][1]); SB0();
    WLD(wf[19], vL, ws[1] + 1 * WP, -1024); SB0();
    MM(aI[0][0][1], wf[4], xt[1][0][1][1], aI[0][0][1]); SB0();
    MM(aI[0][0][1], wf[5], xt[1][0][0][1], aI[0][0][1]); SB0();
    asm volatile("s_waitcnt lgkmcnt(0)" ::: "memory");
    BAR(); SB0();
    STAMP(4);
    XRD(2, 0, 0, 0);
    XRD(2, 0, 1, 0);
    XRD(2, 0, 0, 1);
    XRD(2, 0, 1, 1);
    XRD(2, 1, 0, 0);
    XRD(2, 1, 1, 0);
    XRD(2, 1, 0, 1);
    XRD(2, 1, 1, 1);
    SB0();
    WAITV(12, wf[6], wf[7]); SB0();
    asm volatile("" : XC_(xt[1][1][0][0]), XC_(xt[1][1][1][0])); SB0();
    MM(aP[0][1][0], wf[6], xt[1][1][0][0], aP[0][1][0]); SB0();
    WLD(wf[20], vL, ws[1] + 1 * WP, 0); SB0();
    MM(aQ[0][1][0], wf[7], xt[1][1][1][0], aQ[0][1][0]); SB0();
    MM(aI[0][1][0], wf[6], xt[1][1][1][0], aI[0][1][0]); SB0();
    MM(aI[0][1][0], wf[7], xt[1][1][0][0], aI[0][1][0]); SB0();
    WLD(wf[21], vL, ws[1] + 1 * WP, 1024); SB0();
    asm volatile("" : XC_(xt[1][1][0][1]), XC_(xt[1][1][1][1])); SB0();
    MM(aP[0][1][1], wf[6], xt[1][1][0][1], aP[0][1][1]); SB0();
    MM(aQ[0][1][1], wf[7], xt[1][1][1][1], aQ[0][1][1]); SB0();
    MM(aI[0][1][1], wf[6], xt[1][1][1][1], aI[0][1][1]); SB0();
    MM(aI[0][1][1], wf[7], xt[1][1][0][1], aI[0][1][1]); SB0();
    WAITV(12, wf[8], wf[9]); SB0();
    MM(aP[1][0][0], wf[8], xt[0][0][0][0], Z4); SB0();
    WLD(wf[22], vL, ws[1] + 1 * WP, 2048); SB0();
    MM(aQ[1][0][0], wf[9], xt[0][0][1][0], Z4); SB0();
    MM(aI[1][0][0], wf[8], xt[0][0][1][0], Z4); SB0();
    MM(aI[1][0][0], wf[9], xt[0][0][0][0], aI[1][0][0]); SB0();
    WLD(wf[23], vL, ws[1] + 1 * WP, 3072); SB0();
    MM(aP[1][0][1], wf[8], xt[0][0][0][1], Z4); SB0();
    MM(aQ[1][0][1], wf[9], xt[0][0][1][1], Z4); SB0();
    MM(aI[1][0][1], wf[8], xt[0][0][1][1], Z4); SB0();
    MM(aI[1][0][1], wf[9], xt[0][0][0][1], aI[1][0][1]); SB0();
    WAITV(12, wf[10], wf[11]); SB0();
    asm volatile("s_waitcnt lgkmcnt(6)" : XC_(xt[2][0][0][0]), XC_(xt[2][0][1][0]) :: "memory"); SB0();
    MM(aP[0][0][0], wf[10], xt[2][0][0][0], aP[0][0][0]); SB0();
    WLD(wf[24], vL, ws[0] + 2 * WP, -2048); SB0();
    MM(aQ[0][0][0], wf[11], xt[2][0][1][0], aQ[0][0][0]); SB0();
    MM(aI[0][0][0], wf[10], xt[2][0][1][0], aI[0][0][0]); SB0();
    MM(aI[0][0][0], wf[11], xt[2][0][0][0], aI[0][0][0]); SB0();
    WLD(wf[25], vL, ws[0] + 2 * WP, -1024); SB0();
    asm volatile("s_waitcnt lgkmcnt(4)" : XC_(xt[2][0][0][1]), XC_(xt[2][0][1][1]) :: "memory"); SB0();
    MM(aP[0][0][1], wf[10], xt[2][0][0][1], aP[0][0][1]); SB0();
    MM(aQ[0][0][1], wf[11], xt[2][0][1][1], aQ[0][0][1]); SB0();
    ZC(0, 0, 0, 0); SB0();
    MM(aI[0][0][1], wf[10], xt[2][0][1][1], aI[0][0][1]); SB0();
    ZC(0, 0, 0, 1); SB0();
    MM(aI[0][0][1], wf[11], xt[2][0][0][1], aI[0][0][1]); SB0();
    ZC(0, 0, 0, 2); SB0();
    WAITV(12, wf[12], wf[13]); SB0();
    asm volatile("s_waitcnt lgkmcnt(2)" : XC_(xt[2][1][0][0]), XC_(xt[2][1][1][0]) :: "memory"); SB0();
    MM(aP[0][1][0], wf[12], xt[2][1][0][0], aP[0][1][0]); SB0();
    WLD(wf[26], vL, ws[0] + 2 * WP, 0); SB0();
    ZC(0, 0, 0, 3); SB0();
    MM(aQ[0][1][0], wf[13], xt[2][1][1][0], aQ[0][1][0]); SB0();
    ZW(0, 0, 0); SB0();
    ZC(0, 0, 1, 0); SB0();
    MM(aI[0][1][0], wf[12], xt[2][1][1][0], aI[0][1][0]); SB0();
    ZC(0, 0, 1, 1); SB0();
    MM(aI[0][1][0], wf[13], xt[2][1][0][0], aI[0][1][0]); SB0();
    WLD(wf[27], vL, ws[0] + 2 * WP, 1024); SB0();
    ZC(0, 0, 1, 2); SB0();
    asm volatile("s_waitcnt lgkmcnt(1)" : XC_(xt[2][1][0][1]), XC_(xt[2][1][1][1]) :: "memory"); SB0();
    MM(aP[0][1][1], wf[12], xt[2][1][0][1], aP[0][1][1]); SB0();
    ZC(0, 0, 1, 3); SB0();
    ZW(0, 0, 1); SB0();
    MM(aQ[0][1][1], wf[13], xt[2][1][1][1], aQ[0][1][1]); SB0();
    ZC(0, 1, 0, 0); SB0();
    MM(aI[0][1][1], wf[12], xt[2][1][1][1], aI[0][1][1]); SB0();
    ZC(0, 1, 0, 1); SB0();
    MM(aI[0][1][1], wf[13], xt[2][1][0][1], aI[0][1][1]); SB0();
    ZC(0, 1, 0, 2); SB0();
    WAITV(12, wf[14], wf[15]); SB0();
    MM(aP[1][0][0], wf[14], xt[1][0][0][0], aP[1][0][0]); SB0();
    WLD(wf[28], vL, ws[0] + 2 * WP, 2048); SB0();
    ZC(0, 1, 0, 3); SB0();
    MM(aQ[1][0][0], wf[15], xt[1][0][1][0], aQ[1][0][0]); SB0();
    ZW(0, 1, 0); SB0();
    ZC(0, 1, 1, 0); SB0();
    MM(aI[1][0][0], wf[14], xt[1][0][1][0], aI[1][0][0]); SB0();
    ZC(0, 1, 1, 1); SB0();
    MM(aI[1][0][0], wf[15], xt[1][0][0][0], aI[1][0][0]); SB0();
    WLD(wf[29], vL, ws[0] + 2 * WP, 3072); SB0();
    ZC(0, 1, 1, 2); SB0();
    MM(aP[1][0][1], wf[14], xt[1][0][0][1], aP[1][0][1]); SB0();
    ZC(0, 1, 1, 3); SB0();
    ZW(0, 1, 1); SB0();
    MM(aQ[1][0][1], wf[15], xt[1][0][1][1], aQ[1][0][1]); SB0();
    MM(aI[1][0][1], wf[14], xt[1][0][1][1], aI[1][0][1]); SB0();
    MM(aI[1][0][1], wf[15], xt[1][0][0][1], aI[1][0][1]); SB0();
    STAMP(12);
    LGW0(); BAR(); SB0();
    STAMP(13);
    TABI();
    ZRD(0, 0); ZRD(1, 2 * M_ZT); SB0();
    WAITV(12, wf[16], wf[17]); SB0();
    MM(aP[1][0][0], wf[16], xt[2][0][0][0], aP[1][0][0]); SB0();
    WLD(wf[30], vL, ws[1] + 2 * WP, -2048); SB0();
    MM(aQ[1][0][0], wf[17], xt[2][0][1][0], aQ[1][0][0]); SB0();
    MM(aI[1][0][0], wf[16], xt[2][0][1][0], aI[1][0][0]); SB0();
    MM(aI[1][0][0], wf[17], xt[2][0][0][0], aI[1][0][0]); SB0();
    WLD(wf[31], vL, ws[1] + 2 * WP, -1024); SB0();
    ZIW(0, 4); SB0();
    asm volatile("" : XC_(fi[0])); SB0();
    ZIM(0, 0); SB0();
    ZIM(0, 1); SB0();
    ZC(1, 0, 0, 0); SB0();
    ZIW(1, 0); SB0();
    ZIM(1, 0); SB0();
    ZC(1, 0, 0, 1); SB0();
    ZIM(1, 1); SB0();
    ZC(1, 0, 0, 2); SB0();
    MM(aP[1][0][1], wf[16], xt[2][0][0][1], aP[1][0][1]); SB0();
    MM(aQ[1][0][1], wf[17], xt[2][0][1][1], aQ[1][0][1]); SB0();
    ZIC(0, 0); SB0();
    MM(aI[1][0][1], wf[16], xt[2][0][1][1], aI[1][0][1]); SB0();
    ZIC(0, 1); SB0();
    MM(aI[1][0][1], wf[17], xt[2][0][0][1], aI[1][0][1]); SB0();
    ZIC(0, 2); SB0();
    WAITV(12, wf[18], wf[19]); SB0();
    MM(aP[1][1][0], wf[18], xt[0][1][0][0], Z4); SB0();
    WLD(wf[32], vL, ws[1] + 2 * WP, 0); SB0();
    YST(zo, ys + 0 * YT + 0 * YG, 0); SB0();
    MM(aQ[1][1][0], wf[19], xt[0][1][1][0], Z4); SB0();
    ZIC(1, 0); SB0();
    MM(aI[1][1][0], wf[18], xt[0][1][1][0], Z4); SB0();
    ZIC(1, 1); SB0();
    MM(aI[1][1][0], wf[19], xt[0][1][0][0], aI[1][1][0]); SB0();
    WLD(wf[33], vL, ws[1] + 2 * WP, 1024); SB0();
    ZIC(1, 2); SB0();
    MM(aP[1][1][1], wf[18], xt[0][1][0][1], Z4); SB0();
    YST(zo, ys + 2 * YT + 0 * YG, 1); SB0();
    ZC(1, 0, 0, 3); SB0();
    MM(aQ[1][1][1], wf[19], xt[0][1][1][1], Z4); SB0();
    ZW(1, 0, 0); SB0();
    ZC(1, 0, 1, 0); SB0();
    MM(aI[1][1][1], wf[18], xt[0][1][1][1], Z4); SB0();
    ZC(1, 0, 1, 1); SB0();
    MM(aI[1][1][1], wf[19], xt[0][1][0][1], aI[1][1][1]); SB0();
    ZC(1, 0, 1, 2); SB0();
    WAITV(14, wf[20], wf[21]); SB0();
    MM(aP[1][1][0], wf[20], xt[1][1][0][0], aP[1][1][0]); SB0();
    WLD(wf[34], vL, ws[1] + 2 * WP, 2048); SB0();
    ZC(1, 0, 1, 3); SB0();
    MM(aQ[1][1][0], wf[21], xt[1][1][1][0], aQ[1][1][0]); SB0();
    ZW(1, 0, 1); SB0();
    MM(aI[1][1][0], wf[20], xt[1][1][1][0], aI[1][1][0]); SB0();
    MM(aI[1][1][0], wf[21], xt[1][1][0][0], aI[1][1][0]); SB0();
    WLD(wf[35], vL, ws[1] + 2 * WP, 3072); SB0();
    MM(aP[1][1][1], wf[20], xt[1][1][0][1], aP[1][1][1]); SB0();
    MM(aQ[1][1][1], wf[21], xt[1][1][1][1], aQ[1][1][1]); SB0();
    MM(aI[1][1][1], wf[20], xt[1][1][1][1], aI[1][1][1]); SB0();
    MM(aI[1][1][1], wf[21], xt[1][1][0][1], aI[1][1][1]); SB0();
    WAITV(14, wf[22], wf[23]); SB0();
    MM(aP[1][1][0], wf[22], xt[2][1][0][0], aP[1][1][0]); SB0();
    MM(aQ[1][1][0], wf[23], xt[2][1][1][0], aQ[1][1][0]); SB0();
    MM(aI[1][1][0], wf[22], xt[2][1][1][0], aI[1][1][0]); SB0();
    MM(aI[1][1][0], wf[23], xt[2][1][0][0], aI[1][1][0]); SB0();
    MM(aP[1][1][1], wf[22], xt[2][1][0][1], aP[1][1][1]); SB0();
    MM(aQ[1][1][1], wf[23], xt[2][1][1][1], aQ[1][1][1]); SB0();
    ZC(1, 1, 0, 0); SB0();
    MM(aI[1][1][1], wf[22], xt[2][1][1][1], aI[1][1][1]); SB0();
    ZC(1, 1, 0, 1); SB0();
    MM(aI[1][1][1], wf[23], xt[2][1][0][1], aI[1][1][1]); SB0();
    ZC(1, 1, 0, 2); SB0();
    WAITV(12, wf[24], wf[25]); SB0();
    MM(aP[2][0][0], wf[24], xt[0][0][0][0], Z4); SB0();
    ZC(1, 1, 0, 3); SB0();
    ZW(1, 1, 0); SB0();
    MM(aQ[2][0][0], wf[25], xt[0][0][1][0], Z4); SB0();
    ZC(1, 1, 1, 0); SB0();
    MM(aI[2][0][0], wf[24], xt[0][0][1][0], Z4); SB0();
    ZC(1, 1, 1, 1); SB0();
    MM(aI[2][0][0], wf[25], xt[0][0][0][0], aI[2][0][0]); SB0();
    ZC(1, 1, 1, 2); SB0();
    MM(aP[2][0][1], wf[24], xt[0][0][0][1], Z4); SB0();
    ZC(1, 1, 1, 3); SB0();
    ZW(1, 1, 1); SB0();
    MM(aQ[2][0][1], wf[25], xt[0][0][1][1], Z4); SB0();
    MM(aI[2][0][1], wf[24], xt[0][0][1][1], Z4); SB0();
    MM(aI[2][0][1], wf[25], xt[0][0][0][1], aI[2][0][1]); SB0();
    STAMP(14);
    LGW0(); BAR(); SB0();
    STAMP(15);
    ZRD(0, M_Z1 + 0); ZRD(1, M_Z1 + 2 * M_ZT); SB0();
    WAITV(10, wf[26], wf[27]); SB0();
    MM(aP[2][0][0], wf[26], xt[1][0][0][0], aP[2][0][0]); SB0();
    VMW(2); ARRY(0); SB0();
    MM(aQ[2][0][0], wf[27], xt[1][0][1][0], aQ[2][0][0]); SB0();
    MM(aI[2][0][0], wf[26], xt[1][0][1][0], aI[2][0][0]); SB0();
    MM(aI[2][0][0], wf[27], xt[1][0][0][0], aI[2][0][0]); SB0();
    ZIW(0, 4); SB0();
    ZIM(0, 0); SB0();
    ZIM(0, 1); SB0();
    ZIW(1, 0); SB0();
    ZIM(1, 0); SB0();
    ZIM(1, 1); SB0();
    MM(aP[2][0][1], wf[26], xt[1][0][0][1], aP[2][0][1]); SB0();
    MM(aQ[2][0][1], wf[27], xt[1][0][1][1], aQ[2][0][1]); SB0();
    ZIC(0, 0); SB0();
    MM(aI[2][0][1], wf[26], xt[1][0][1][1], aI[2][0][1]); SB0();
    ZIC(0, 1); SB0();
    MM(aI[2][0][1], wf[27], xt[1][0][0][1], aI[2][0][1]); SB0();
    ZIC(0, 2); SB0();
    WAITV(9, wf[28], wf[29]); SB0();
    MM(aP[2][0][0], wf[28], xt[2][0][0][0], aP[2][0][0]); SB0();
    YST(zo, ys + 0 * YT + 1 * YG, 0); SB0();
    ZIC(1, 0); SB0();
    MM(aQ[2][0][0], wf[29], xt[2][0][1][0], aQ[2][0][0]); SB0();
    ZIC(1, 1); SB0();
    MM(aI[2][0][0], wf[28], xt[2][0][1][0], aI[2][0][0]); SB0();
    ZIC(1, 2); SB0();
    MM(aI[2][0][0], wf[29], xt[2][0][0][0], aI[2][0][0]); SB0();
    YST(zo, ys + 2 * YT + 1 * YG, 1); SB0();
    MM(aP[2][0][1], wf[28], xt[2][0][0][1], aP[2][0][1]); SB0();
    MM(aQ[2][0][1], wf[29], xt[2][0][1][1], aQ[2][0][1]); SB0();
    ZC(2, 0, 0, 0); SB0();
    MM(aI[2][0][1], wf[28], xt[2][0][1][1], aI[2][0][1]); SB0();
    ZC(2, 0, 0, 1); SB0();
    MM(aI[2][0][1], wf[29], xt[2][0][0][1], aI[2][0][1]); SB0();
    ZC(2, 0, 0, 2); SB0();
    WAITV(9, wf[30], wf[31]); SB0();
    MM(aP[2][1][0], wf[30], xt[0][1][0][0], Z4); SB0();
    ZC(2, 0, 0, 3); SB0();
    ZW(2, 0, 0); SB0();
    MM(aQ[2][1][0], wf[31], xt[0][1][1][0], Z4); SB0();
    ZC(2, 0, 1, 0); SB0();
    MM(aI[2][1][0], wf[30], xt[0][1][1][0], Z4); SB0();
    ZC(2, 0, 1, 1); SB0();
    MM(aI[2][1][0], wf[31], xt[0][1][0][0], aI[2][1][0]); SB0();
    ZC(2, 0, 1, 2); SB0();
    MM(aP[2][1][1], wf[30], xt[0][1][0][1], Z4); SB0();
    ZC(2, 0, 1, 3); SB0();
    ZW(2, 0, 1); SB0();
    MM(aQ[2][1][1], wf[31], xt[0][1][1][1], Z4); SB0();
    MM(aI[2][1][1], wf[30], xt[0][1][1][1], Z4); SB0();
    MM(aI[2][1][1], wf[31], xt[0][1][0][1], aI[2][1][1]); SB0();
    WAITV(6, wf[32], wf[33]); SB0();
    MM(aP[2][1][0], wf[32], xt[1][1][0][0], aP[2][1][0]); SB0();
    MM(aQ[2][1][0], wf[33], xt[1][1][1][0], aQ[2][1][0]); SB0();
    MM(aI[2][1][0], wf[32], xt[1][1][1][0], aI[2][1][0]); SB0();
    MM(aI[2][1][0], wf[33], xt[1][1][0][0], aI[2][1][0]); SB0();
    MM(aP[2][1][1], wf[32], xt[1][1][0][1], aP[2][1][1]); SB0();
    MM(aQ[2][1][1], wf[33], xt[1][1][1][1], aQ[2][1][1]); SB0();
    MM(aI[2][1][1], wf[32], xt[1][1][1][1], aI[2][1][1]); SB0();
    MM(aI[2][1][1], wf[33], xt[1][1][0][1], aI[2][1][1]); SB0();
    WAITV(3, wf[34], wf[35]); SB0();
    MM(aP[2][1][0], wf[34], xt[2][1][0][0], aP[2][1][0]); SB0();
    MM(aQ[2][1][0], wf[35], xt[2][1][1][0], aQ[2][1][0]); SB0();
    MM(aI[2][1][0], wf[34], xt[2][1][1][0], aI[2][1][0]); SB0();
    MM(aI[2][1][0], wf[35], xt[2][1][0][0], aI[2][1][0]); SB0();
    MM(aP[2][1][1], wf[34], xt[2][1][0][1], aP[2][1][1]); SB0();
    MM(aQ[2][1][1], wf[35], xt[2][1][1][1], aQ[2][1][1]); SB0();
    ZC(2, 1, 0, 0); SB0();
    MM(aI[2][1][1], wf[34], xt[2][1][1][1], aI[2][1][1]); SB0();
    ZC(2, 1, 0, 1); SB0();
    MM(aI[2][1][1], wf[35], xt[2][1][0][1], aI[2][1][1]); SB0();
    ZC(2, 1, 0, 2); SB0();
    STAMP(5);
    ZC(2, 1, 0, 3); SB0();
    ZW(2, 1, 0); SB0();
    ZC(2, 1, 1, 0); SB0();
    ZC(2, 1, 1, 1); SB0();
    ZC(2, 1, 1, 2); SB0();
    ZC(2, 1, 1, 3); SB0();
    ZW(2, 1, 1); SB0();
    VMW(0); ARRY(1); SB0();
    STAMP(1);
    STAMP(2);
    LGW0(); BAR(); SB0();
    } else {
    XPIN();
    __builtin_amdgcn_s_setprio(3);
    XLD(0, 0, 0); SB0();
    XLD(0, 1, 0); SB0();
    TABF();
    XLD(0, 0, 1); SB0();
    XLD(0, 1, 1); SB0();
    XLD(0, 0, 2); SB0();
    XLD(0, 1, 2); SB0();
    __builtin_amdgcn_s_setprio(2);
    SETUP_X(); SB0();
    XLD(1, 0, 0); SB0();
    XLD(1, 1, 0); SB0();
    XLD(1, 0, 1); SB0();
    XLD(1, 1, 1); SB0();
    XLD(1, 0, 2); SB0();
    XLD(1, 1, 2); SB0();
    __builtin_amdgcn_s_setprio(0);
    SB0(); SETUP_P(); SETUP_M(); SETUP_Z(); SB0();
    VMW(6); SB0();
    asm volatile("" : XC_(fh[0][0]), XC_(fh[1][0])); SB0();
    STAMP(1);
    TDV(0); SB0();
    TMF(0, 0, 0); SB0();
    TMF(0, 1, 0); SB0();
    XLD(2, 0, 0); SB0();
    TMF(0, 0, 1); SB0();
    TMF(0, 1, 1); SB0();
    XLD(2, 1, 0); SB0();
    TMF(1, 0, 0); SB0();
    TCV(0, 0, 0); SB0();
    TCV(1, 0, 1); SB0();
    TM0(0);
    TWR(0, 0, 0, 0); SB0();
    TCV(2, 0, 2); SB0();
    TMF(1, 1, 0); SB0();
    XLD(2, 0, 1); SB0();
    TWR(1, 0, 0, 1); SB0();
    TCV(3, 0, 3); SB0();
    TWR(2, 0, 0, 2); SB0();
    TMF(1, 0, 1); SB0();
    TCV(0, 0, 4); SB0();
    TWR(3, 0, 0, 3); SB0();
    TCV(1, 0, 5); SB0();
    TWR(0, 0, 0, 4); SB0();
    TMF(1, 1, 1); SB0();
    XLD(2, 1, 1); SB0();
    TCV(2, 0, 6); SB0();
    TWR(1, 0, 0, 5); SB0();
    TWR(2, 0, 0, 6); SB0();
    TMF(2, 0, 0); SB0();
    TCV(3, 1, 0); SB0();
    TCV(0, 1, 1); SB0();
    TWR(3, 0, 1, 0); SB0();
    TCV(1, 1, 2); SB0();
    TMF(2, 1, 0); SB0();
    XLD(2, 0, 2); SB0();
    TWR(0, 0, 1, 1); SB0();
    TCV(2, 1, 3); SB0();
    TWR(1, 0, 1, 2); SB0();
    TMF(2, 0, 1); SB0();
    TCV(3, 1, 4); SB0();
    TWR(2, 0, 1, 3); SB0();
    TCV(0, 1, 5); SB0();
    TWR(3, 0, 1, 4); SB0();
    TMF(2, 1, 1); SB0();
    XLD(2, 1, 2); SB0();
    TCV(1, 1, 6); SB0();
    TWR(0, 0, 1, 5); SB0();
    TWR(1, 0, 1, 6); SB0();
    POLLW(); SB0();
    TMF(3, 0, 0); SB0();
    TCV(2, 2, 0); SB0();
    TCV(3, 2, 1); SB0();
    TWR(2, 0, 2, 0); SB0();
    TCV(0, 2, 2); SB0();
    TMF(3, 1, 0); SB0();
    TWR(3, 0, 2, 1); SB0();
    TCV(1, 2, 3); SB0();
    TWR(0, 0, 2, 2); SB0();
    TMF(3, 0, 1); SB0();
    TABD(); SB0();
    TCV(2, 2, 4); SB0();
    TM0(0);
    TWR(1, 0, 2, 3); SB0();
    TCV(3, 2, 5); SB0();
    TMF(3, 1, 1); SB0();
    TWR(2, 0, 2, 4); SB0();
    TCV(0, 2, 6); SB0();
    TWR(3, 0, 2, 5); SB0();
    TWR(0, 0, 2, 6); SB0();
    VMW(8); SB0();
    TDV(1); SB0();
    TMF(0, 0, 0); SB0();
    TCV(1, 3, 0); SB0();
    TCV(2, 3, 1); SB0();
    TWR(1, 0, 3, 0); SB0();
    TCV(3, 3, 2); SB0();
    TMF(0, 1, 0); SB0();
    TWR(2, 0, 3, 1); SB0();
    TCV(0, 3, 3); SB0();
    TWR(3, 0, 3, 2); SB0();
    TMF(0, 0, 1); SB0();
    TCV(1, 3, 4); SB0();
    TWR(0, 0, 3, 3); SB0();
    TCV(2, 3, 5); SB0();
    TWR(1, 0, 3, 4); SB0();
    TMF(0, 1, 1); SB0();
    TCV(3, 3, 6); SB0();
    TWR(2, 0, 3, 5); SB0();
    TWR(3, 0, 3, 6); SB0();
    TMF(1, 0, 0); SB0();
    TCV(0, 0, 0); SB0();
    TCV(1, 0, 1); SB0();
    TM0(1);
    TWR(0, 1, 0, 0); SB0();
    TCV(2, 0, 2); SB0();
    TMF(1, 1, 0); SB0();
    TWR(1, 1, 0, 1); SB0();
    TCV(3, 0, 3); SB0();
    TWR(2, 1, 0, 2); SB0();
    TMF(1, 0, 1); SB0();
    TCV(0, 0, 4); SB0();
    TWR(3, 1, 0, 3); SB0();
    TCV(1, 0, 5); SB0();
    TWR(0, 1, 0, 4); SB0();
    TMF(1, 1, 1); SB0();
    TCV(2, 0, 6); SB0();
    TWR(1, 1, 0, 5); SB0();
    TWR(2, 1, 0, 6); SB0();
    CHECKW(1); SB0();
    asm volatile("s_waitcnt lgkmcnt(7)" ::: "memory");
    BAR(); SB0();
    STAMP(2);
    WLD(wf[0], vL, ws[0] + 0 * WP, -2048); SB0();
    WLD(wf[1], vL, ws[0] + 0 * WP, -1024); SB0();
    WLD(wf[2], vL, ws[1] + 0 * WP, -2048); SB0();
    WLD(wf[3], vL, ws[1] + 0 * WP, -1024); SB0();
    XRD(0, 0, 0, 0);
    XRD(0, 0, 1, 0);
    XRD(0, 0, 0, 1);
    XRD(0, 0, 1, 1);
    XRD(0, 1, 0, 0);
    XRD(0, 1, 1, 0);
    SB0();
    TMF(2, 0, 0); SB0();
    WLD(wf[4], vL, ws[0] + 0 * WP, 0); SB0();
    TCV(3, 1, 0); SB0();
    TCV(0, 1, 1); SB0();
    TWR(3, 1, 1, 0); SB0();
    TMF(2, 1, 0); SB0();
    TCV(1, 1, 2); SB0();
    TWR(0, 1, 1, 1); SB0();
    TCV(2, 1, 3); SB0();
    TWR(1, 1, 1, 2); SB0();
    TMF(2, 0, 1); SB0();
    TCV(3, 1, 4); SB0();
    TWR(2, 1, 1, 3); SB0();
    TCV(0, 1, 5); SB0();
    TWR(3, 1, 1, 4); SB0();
    TMF(2, 1, 1); SB0();
    WLD(wf[5], vL, ws[0] + 0 * WP, 1024); SB0();
    TCV(1, 1, 6); SB0();
    TWR(0, 1, 1, 5); SB0();
    TWR(1, 1, 1, 6); SB0();
    TMF(3, 0, 0); SB0();
    TCV(2, 2, 0); SB0();
    TCV(3, 2, 1); SB0();
    TWR(2, 1, 2, 0); SB0();
    TCV(0, 2, 2); SB0();
    TMF(3, 1, 0); SB0();
    TWR(3, 1, 2, 1); SB0();
    TCV(1, 2, 3); SB0();
    TWR(0, 1, 2, 2); SB0();
    TMF(3, 0, 1); SB0();
    WLD(wf[6], vL, ws[1] + 0 * WP, 0); SB0();
    TCV(2, 2, 4); SB0();
    TWR(1, 1, 2, 3); SB0();
    TCV(3, 2, 5); SB0();
    TMF(3, 1, 1); SB0();
    TWR(2, 1, 2, 4); SB0();
    TCV(0, 2, 6); SB0();
    TWR(3, 1, 2, 5); SB0();
    TWR(0, 1, 2, 6); SB0();
    TCV(1, 3, 0); SB0();
    TCV(2, 3, 1); SB0();
    TWR(1, 1, 3, 0); SB0();
    TCV(3, 3, 2); SB0();
    TWR(2, 1, 3, 1); SB0();
    TCV(0, 3, 3); SB0();
    TWR(3, 1, 3, 2); SB0();
    TCV(1, 3, 4); SB0();
    TWR(0, 1, 3, 3); SB0();
    TCV(2, 3, 5); SB0();
    TWR(1, 1, 3, 4); SB0();
    TCV(3, 3, 6); SB0();
    TWR(2, 1, 3, 5); SB0();
    TWR(3, 1, 3, 6); SB0();
    asm volatile("s_waitcnt lgkmcnt(0)" ::: "memory");
    BAR(); SB0();
    STAMP(3);
    XRD(1, 0, 0, 0);
    XRD(1, 0, 1, 0);
    XRD(1, 0, 0, 1);
    XRD(1, 0, 1, 1);
    XRD(1, 1, 0, 0);
    XRD(1, 1, 1, 0);
    SB0();
    VMW(9); SB0();
    TDV(2); SB0();
    TMF(0, 0, 0); SB0();
    TMF(0, 1, 0); SB0();
    WLD(wf[7], vL, ws[1] + 0 * WP, 1024); SB0();
    TMF(0, 0, 1); SB0();
    TMF(0, 1, 1); SB0();
    WAITV(6, wf[0], wf[1]); SB0();
    asm volatile("" : XC_(xt[0][0][0][0]), XC_(xt[0][0][1][0])); SB0();
    MM(aP[0][0][0], wf[0], xt[0][0][0][0], Z4); SB0();
    WLD(wf[8], vL, ws[0] + 1 * WP, -2048); SB0();
    TCV(0, 0, 0); SB0();
    TCV(1, 0, 1); SB0();
    MM(aQ[0][0][0], wf[1], xt[0][0][1][0], Z4); SB0();
    TM0(2);
    TWR(0, 2, 0, 0); SB0();
    TCV(2, 0, 2); SB0();
    TWR(1, 2, 0, 1); SB0();
    MM(aI[0][0][0], wf[0], xt[0][0][1][0], Z4); SB0();
    TCV(3, 0, 3); SB0();
    TWR(2, 2, 0, 2); SB0();
    TCV(0, 0, 4); SB0();
    MM(aI[0][0][0], wf[1], xt[0][0][0][0], aI[0][0][0]); SB0();
    WLD(wf[9], vL, ws[0] + 1 * WP, -1024); SB0();
    TWR(3, 2, 0, 3); SB0();
    TCV(1, 0, 5); SB0();
    TMF(1, 0, 0); SB0();
    TWR(0, 2, 0, 4); SB0();
    TCV(2, 0, 6); SB0();
    TWR(1, 2, 0, 5); SB0();
    TMF(1, 1, 0); SB0();
    TWR(2, 2, 0, 6); SB0();
    TMF(1, 0, 1); SB0();
    WLD(wf[10], vL, ws[0] + 0 * WP, 2048); SB0();
    TMF(1, 1, 1); SB0();
    asm volatile("" : XC_(xt[0][0][0][1]), XC_(xt[0][0][1][1])); SB0();
    MM(aP[0][0][1], wf[0], xt[0][0][0][1], Z4); SB0();
    TCV(3, 1, 0); SB0();
    TCV(0, 1, 1); SB0();
    TWR(3, 2, 1, 0); SB0();
    MM(aQ[0][0][1], wf[1], xt[0][0][1][1], Z4); SB0();
    WLD(wf[11], vL, ws[0] + 0 * WP, 3072); SB0();
    TCV(1, 1, 2); SB0();
    TWR(0, 2, 1, 1); SB0();
    MM(aI[0][0][1], wf[0], xt[0][0][1][1], Z4); SB0();
    TCV(2, 1, 3); SB0();
    TWR(1, 2, 1, 2); SB0();
    TCV(3, 1, 4); SB0();
    MM(aI[0][0][1], wf[1], xt[0][0][0][1], aI[0][0][1]); SB0();
    TWR(2, 2, 1, 3); SB0();
    TCV(0, 1, 5); SB0();
    TWR(3, 2, 1, 4); SB0();
    TMF(2, 0, 0); SB0();
    WLD(wf[12], vL, ws[1] + 0 * WP, 2048); SB0();
    TCV(1, 1, 6); SB0();
    TWR(0, 2, 1, 5); SB0();
    TMF(2, 1, 0); SB0();
    TWR(1, 2, 1, 6); SB0();
    TMF(2, 0, 1); SB0();
    TMF(2, 1, 1); SB0();
    WLD(wf[13], vL, ws[1] + 0 * WP, 3072); SB0();
    WAITV(10, wf[2], wf[3]); SB0();
    asm volatile("" : XC_(xt[0][1][0][0]), XC_(xt[0][1][1][0])); SB0();
    MM(aP[0][1][0], wf[2], xt[0][1][0][0], Z4); SB0();
    TCV(2, 2, 0); SB0();
    TCV(3, 2, 1); SB0();
    TWR(2, 2, 2, 0); SB0();
    MM(aQ[0][1][0], wf[3], xt[0][1][1][0], Z4); SB0();
    TCV(0, 2, 2); SB0();
    TWR(3, 2, 2, 1); SB0();
    TCV(1, 2, 3); SB0();
    MM(aI[0][1][0], wf[2], xt[0][1][1][0], Z4); SB0();
    WLD(wf[14], vL, ws[0] + 1 * WP, 0); SB0();
    TWR(0, 2, 2, 2); SB0();
    TCV(2, 2, 4); SB0();
    MM(aI[0][1][0], wf[3], xt[0][1][0][0], aI[0][1][0]); SB0();
    TWR(1, 2, 2, 3); SB0();
    TCV(3, 2, 5); SB0();
    TWR(2, 2, 2, 4); SB0();
    TMF(3, 0, 0); SB0();
    TCV(0, 2, 6); SB0();
    TWR(3, 2, 2, 5); SB0();
    TWR(0, 2, 2, 6); SB0();
    TMF(3, 1, 0); SB0();
    WLD(wf[15], vL, ws[0] + 1 * WP, 1024); SB0();
    TMF(3, 0, 1); SB0();
    TMF(3, 1, 1); SB0();
    WAITV(10, wf[4], wf[5]); SB0();
    asm volatile("s_waitcnt lgkmcnt(15)" : XC_(xt[1][0][0][0]), XC_(xt[1][0][1][0]) :: "memory"); SB0();
    MM(aP[0][0][0], wf[4], xt[1][0][0][0], aP[0][0][0]); SB0();
    WLD(wf[16], vL, ws[0] + 1 * WP, 2048); SB0();
    TCV(1, 3, 0); SB0();
    TCV(2, 3, 1); SB0();
    MM(aQ[0][0][0], wf[5], xt[1][0][1][0], aQ[0][0][0]); SB0();
    TWR(1, 2, 3, 0); SB0();
    TCV(3, 3, 2); SB0();
    TWR(2, 2, 3, 1); SB0();
    MM(aI[0][0][0], wf[4], xt[1][0][1][0], aI[0][0][0]); SB0();
    TCV(0, 3, 3); SB0();
    TWR(3, 2, 3, 2); SB0();
    TCV(1, 3, 4); SB0();
    MM(aI[0][0][0], wf[5], xt[1][0][0][0], aI[0][0][0]); SB0();
    WLD(wf[17], vL, ws[0] + 1 * WP, 3072); SB0();
    TWR(0, 2, 3, 3); SB0();
    TCV(2, 3, 5); SB0();
    asm volatile("" : XC_(xt[1][0][0][1]), XC_(xt[1][0][1][1])); SB0();
    MM(aP[0][0][1], wf[4], xt[1][0][0][1], aP[0][0][1]); SB0();
    TWR(1, 2, 3, 4); SB0();
    TCV(3, 3, 6); SB0();
    TWR(2, 2, 3, 5); SB0();
    MM(aQ[0][0][1], wf[5], xt[1][0][1][1], aQ[0][0][1]); SB0();
    TWR(3, 2, 3, 6); SB0();
    MM(aI[0][0][1], wf[4], xt[1][0][1][1], aI[0][0][1]); SB0();
    WLD(wf[18], vL, ws[1] + 1 * WP, -2048); SB0();
    MM(aI[0][0][1], wf[5], xt[1][0][0][1], aI[0][0][1]); SB0();
    asm volatile("s_waitcnt lgkmcnt(0)" ::: "memory");
    BAR(); SB0();
    STAMP(4);
    XRD(2, 0, 0, 0);
    XRD(2, 0, 1, 0);
    XRD(2, 0, 0, 1);
    XRD(2, 0, 1, 1);
    XRD(2, 1, 0, 0);
    XRD(2, 1, 1, 0);
    SB0();
    WAITV(11, wf[6], wf[7]); SB0();
    asm volatile("" : XC_(xt[1][1][0][0]), XC_(xt[1][1][1][0])); SB0();
    MM(aP[0][1][0], wf[6], xt[1][1][0][0], aP[0][1][0]); SB0();
    MM(aQ[0][1][0], wf[7], xt[1][1][1][0], aQ[0][1][0]); SB0();
    WLD(wf[19], vL, ws[1] + 1 * WP, -1024); SB0();
    MM(aI[0][1][0], wf[6], xt[1][1][1][0], aI[0][1][0]); SB0();
    MM(aI[0][1][0], wf[7], xt[1][1][0][0], aI[0][1][0]); SB0();
    WAITV(10, wf[8], wf[9]); SB0();
    MM(aP[1][0][0], wf[8], xt[0][0][0][0], Z4); SB0();
    WLD(wf[20], vL, ws[1] + 1 * WP, 0); SB0();
    MM(aQ[1][0][0], wf[9], xt[0][0][1][0], Z4); SB0();
    MM(aI[1][0][0], wf[8], xt[0][0][1][0], Z4); SB0();
    MM(aI[1][0][0], wf[9], xt[0][0][0][0], aI[1][0][0]); SB0();
    WLD(wf[21], vL, ws[1] + 1 * WP, 1024); SB0();
    MM(aP[1][0][1], wf[8], xt[0][0][0][1], Z4); SB0();
    MM(aQ[1][0][1], wf[9], xt[0][0][1][1], Z4); SB0();
    MM(aI[1][0][1], wf[8], xt[0][0][1][1], Z4); SB0();
    WLD(wf[22], vL, ws[1] + 1 * WP, 2048); SB0();
    MM(aI[1][0][1], wf[9], xt[0][0][0][1], aI[1][0][1]); SB0();
    WAITV(11, wf[10], wf[11]); SB0();
    asm volatile("s_waitcnt lgkmcnt(4)" : XC_(xt[2][0][0][0]), XC_(xt[2][0][1][0]) :: "memory"); SB0();
    MM(aP[0][0][0], wf[10], xt[2][0][0][0], aP[0][0][0]); SB0();
    MM(aQ[0][0][0], wf[11], xt[2][0][1][0], aQ[0][0][0]); SB0();
    WLD(wf[23], vL, ws[1] + 1 * WP, 3072); SB0();
    MM(aI[0][0][0], wf[10], xt[2][0][1][0], aI[0][0][0]); SB0();
    MM(aI[0][0][0], wf[11], xt[2][0][0][0], aI[0][0][0]); SB0();
    asm volatile("s_waitcnt lgkmcnt(2)" : XC_(xt[2][0][0][1]), XC_(xt[2][0][1][1]) :: "memory"); SB0();
    MM(aP[0][0][1], wf[10], xt[2][0][0][1], aP[0][0][1]); SB0();
    WLD(wf[24], vL, ws[0] + 2 * WP, -2048); SB0();
    MM(aQ[0][0][1], wf[11], xt[2][0][1][1], aQ[0][0][1]); SB0();
    ZC(0, 0, 0, 0); SB0();
    MM(aI[0][0][1], wf[10], xt[2][0][1][1], aI[0][0][1]); SB0();
    ZC(0, 0, 0, 1); SB0();
    MM(aI[0][0][1], wf[11], xt[2][0][0][1], aI[0][0][1]); SB0();
    WLD(wf[25], vL, ws[0] + 2 * WP, -1024); SB0();
    ZC(0, 0, 0, 2); SB0();
    WAITV(12, wf[12], wf[13]); SB0();
    asm volatile("s_waitcnt lgkmcnt(0)" : XC_(xt[2][1][0][0]), XC_(xt[2][1][1][0]) :: "memory"); SB0();
    MM(aP[0][1][0], wf[12], xt[2][1][0][0], aP[0][1][0]); SB0();
    ZC(0, 0, 0, 3); SB0();
    ZW(0, 0, 0); SB0();
    MM(aQ[0][1][0], wf[13], xt[2][1][1][0], aQ[0][1][0]); SB0();
    ZC(0, 0, 1, 0); SB0();
    MM(aI[0][1][0], wf[12], xt[2][1][1][0], aI[0][1][0]); SB0();
    WLD(wf[26], vL, ws[0] + 2 * WP, 0); SB0();
    ZC(0, 0, 1, 1); SB0();
    MM(aI[0][1][0], wf[13], xt[2][1][0][0], aI[0][1][0]); SB0();
    ZC(0, 0, 1, 2); SB0();
    WAITV(11, wf[14], wf[15]); SB0();
    MM(aP[1][0][0], wf[14], xt[1][0][0][0], aP[1][0][0]); SB0();
    ZC(0, 0, 1, 3); SB0();
    ZW(0, 0, 1); SB0();
    MM(aQ[1][0][0], wf[15], xt[1][0][1][0], aQ[1][0][0]); SB0();
    WLD(wf[27], vL, ws[0] + 2 * WP, 1024); SB0();
    ZC(0, 1, 0, 0); SB0();
    MM(aI[1][0][0], wf[14], xt[1][0][1][0], aI[1][0][0]); SB0();
    ZC(0, 1, 0, 1); SB0();
    MM(aI[1][0][0], wf[15], xt[1][0][0][0], aI[1][0][0]); SB0();
    ZC(0, 1, 0, 2); SB0();
    MM(aP[1][0][1], wf[14], xt[1][0][0][1], aP[1][0][1]); SB0();
    WLD(wf[28], vL, ws[0] + 2 * WP, 2048); SB0();
    ZC(0, 1, 0, 3); SB0();
    MM(aQ[1][0][1], wf[15], xt[1][0][1][1], aQ[1][0][1]); SB0();
    ZW(0, 1, 0); SB0();
    MM(aI[1][0][1], wf[14], xt[1][0][1][1], aI[1][0][1]); SB0();
    MM(aI[1][0][1], wf[15], xt[1][0][0][1], aI[1][0][1]); SB0();
    WLD(wf[29], vL, ws[0] + 2 * WP, 3072); SB0();
    STAMP(12);
    LGW0(); BAR(); SB0();
    STAMP(13);
    TABI();
    ZRD(0, 0); ZRD(1, 2 * M_ZT); SB0();
    WAITV(12, wf[16], wf[17]); SB0();
    MM(aP[1][0][0], wf[16], xt[2][0][0][0], aP[1][0][0]); SB0();
    MM(aQ[1][0][0], wf[17], xt[2][0][1][0], aQ[1][0][0]); SB0();
    MM(aI[1][0][0], wf[16], xt[2][0][1][0], aI[1][0][0]); SB0();
    WLD(wf[30], vL, ws[1] + 2 * WP, -2048); SB0();
    MM(aI[1][0][0], wf[17], xt[2][0][0][0], aI[1][0][0]); SB0();
    ZIW(0, 4); SB0();
    asm volatile("" : XC_(fi[0])); SB0();
    ZIM(0, 0); SB0();
    ZIM(0, 1); SB0();
    WLD(wf[31], vL, ws[1] + 2 * WP, -1024); SB0();
    ZC(1, 0, 0, 0); SB0();
    ZIW(1, 0); SB0();
    ZIM(1, 0); SB0();
    ZC(1, 0, 0, 1); SB0();
    ZIM(1, 1); SB0();
    ZC(1, 0, 0, 2); SB0();
    MM(aP[1][0][1], wf[16], xt[2][0][0][1], aP[1][0][1]); SB0();
    WLD(wf[32], vL, ws[1] + 2 * WP, 0); SB0();
    MM(aQ[1][0][1], wf[17], xt[2][0][1][1], aQ[1][0][1]); SB0();
    ZIC(0, 0); SB0();
    MM(aI[1][0][1], wf[16], xt[2][0][1][1], aI[1][0][1]); SB0();
    ZIC(0, 1); SB0();
    MM(aI[1][0][1], wf[17], xt[2][0][0][1], aI[1][0][1]); SB0();
    WLD(wf[33], vL, ws[1] + 2 * WP, 1024); SB0();
    ZIC(0, 2); SB0();
    WAITV(14, wf[18], wf[19]); SB0();
    MM(aP[1][1][0], wf[18], xt[0][1][0][0], Z4); SB0();
    YST(zo, ys + 0 * YT + 0 * YG, 0); SB0();
    ZIC(1, 0); SB0();
    MM(aQ[1][1][0], wf[19], xt[0][1][1][0], Z4); SB0();
    ZIC(1, 1); SB0();
    MM(aI[1][1][0], wf[18], xt[0][1][1][0], Z4); SB0();
    WLD(wf[34], vL, ws[1] + 2 * WP, 2048); SB0();
    ZIC(1, 2); SB0();
    MM(aI[1][1][0], wf[19], xt[0][1][0][0], aI[1][1][0]); SB0();
    YST(zo, ys + 2 * YT + 0 * YG, 1); SB0();
    ZC(1, 0, 0, 3); SB0();
    WAITV(15, wf[20], wf[21]); SB0();
    MM(aP[1][1][0], wf[20], xt[1][1][0][0], aP[1][1][0]); SB0();
    ZW(1, 0, 0); SB0();
    ZC(1, 0, 1, 0); SB0();
    MM(aQ[1][1][0], wf[21], xt[1][1][1][0], aQ[1][1][0]); SB0();
    WLD(wf[35], vL, ws[1] + 2 * WP, 3072); SB0();
    ZC(1, 0, 1, 1); SB0();
    MM(aI[1][1][0], wf[20], xt[1][1][1][0], aI[1][1][0]); SB0();
    ZC(1, 0, 1, 2); SB0();
    MM(aI[1][1][0], wf[21], xt[1][1][0][0], aI[1][1][0]); SB0();
    ZC(1, 0, 1, 3); SB0();
    ZW(1, 0, 1); SB0();
    WAITV(14, wf[22], wf[23]); SB0();
    MM(aP[1][1][0], wf[22], xt[2][1][0][0], aP[1][1][0]); SB0();
    MM(aQ[1][1][0], wf[23], xt[2][1][1][0], aQ[1][1][0]); SB0();
    MM(aI[1][1][0], wf[22], xt[2][1][1][0], aI[1][1][0]); SB0();
    MM(aI[1][1][0], wf[23], xt[2][1][0][0], aI[1][1][0]); SB0();
    WAITV(12, wf[24], wf[25]); SB0();
    MM(aP[2][0][0], wf[24], xt[0][0][0][0], Z4); SB0();
    MM(aQ[2][0][0], wf[25], xt[0][0][1][0], Z4); SB0();
    ZC(1, 1, 0, 0); SB0();
    MM(aI[2][0][0], wf[24], xt[0][0][1][0], Z4); SB0();
    ZC(1, 1, 0, 1); SB0();
    MM(aI[2][0][0], wf[25], xt[0][0][0][0], aI[2][0][0]); SB0();
    ZC(1, 1, 0, 2); SB0();
    MM(aP[2][0][1], wf[24], xt[0][0][0][1], Z4); SB0();
    ZC(1, 1, 0, 3); SB0();
    ZW(1, 1, 0); SB0();
    MM(aQ[2][0][1], wf[25], xt[0][0][1][1], Z4); SB0();
    MM(aI[2][0][1], wf[24], xt[0][0][1][1], Z4); SB0();
    MM(aI[2][0][1], wf[25], xt[0][0][0][1], aI[2][0][1]); SB0();
    STAMP(14);
    LGW0(); BAR(); SB0();
    STAMP(15);
    ZRD(0, M_Z1 + 0); ZRD(1, M_Z1 + 2 * M_ZT); SB0();
    WAITV(10, wf[26], wf[27]); SB0();
    MM(aP[2][0][0], wf[26], xt[1][0][0][0], aP[2][0][0]); SB0();
    MM(aQ[2][0][0], wf[27], xt[1][0][1][0], aQ[2][0][0]); SB0();
    MM(aI[2][0][0], wf[26], xt[1][0][1][0], aI[2][0][0]); SB0();
    MM(aI[2][0][0], wf[27], xt[1][0][0][0], aI[2][0][0]); SB0();
    ZIW(0, 4); SB0();
    ZIM(0, 0); SB0();
    VMW(1); ARRY(0); SB0();
    ZIM(0, 1); SB0();
    ZIW(1, 0); SB0();
    ZIM(1, 0); SB0();
    ZIM(1, 1); SB0();
    MM(aP[2][0][1], wf[26], xt[1][0][0][1], aP[2][0][1]); SB0();
    MM(aQ[2][0][1], wf[27], xt[1][0][1][1], aQ[2][0][1]); SB0();
    ZIC(0, 0); SB0();
    MM(aI[2][0][1], wf[26], xt[1][0][1][1], aI[2][0][1]); SB0();
    ZIC(0, 1); SB0();
    MM(aI[2][0][1], wf[27], xt[1][0][0][1], aI[2][0][1]); SB0();
    ZIC(0, 2); SB0();
    WAITV(9, wf[28], wf[29]); SB0();
    MM(aP[2][0][0], wf[28], xt[2][0][0][0], aP[2][0][0]); SB0();
    YST(zo, ys + 0 * YT + 1 * YG, 0); SB0();
    ZIC(1, 0); SB0();
    MM(aQ[2][0][0], wf[29], xt[2][0][1][0], aQ[2][0][0]); SB0();
    ZIC(1, 1); SB0();
    MM(aI[2][0][0], wf[28], xt[2][0][1][0], aI[2][0][0]); SB0();
    ZIC(1, 2); SB0();
    MM(aI[2][0][0], wf[29], xt[2][0][0][0], aI[2][0][0]); SB0();
    YST(zo, ys + 2 * YT + 1 * YG, 1); SB0();
    MM(aP[2][0][1], wf[28], xt[2][0][0][1], aP[2][0][1]); SB0();
    MM(aQ[2][0][1], wf[29], xt[2][0][1][1], aQ[2][0][1]); SB0();
    ZC(2, 0, 0, 0); SB0();
    MM(aI[2][0][1], wf[28], xt[2][0][1][1], aI[2][0][1]); SB0();
    ZC(2, 0, 0, 1); SB0();
    MM(aI[2][0][1], wf[29], xt[2][0][0][1], aI[2][0][1]); SB0();
    ZC(2, 0, 0, 2); SB0();
    WAITV(9, wf[30], wf[31]); SB0();
    MM(aP[2][1][0], wf[30], xt[0][1][0][0], Z4); SB0();
    ZC(2, 0, 0, 3); SB0();
    ZW(2, 0, 0); SB0();
    MM(aQ[2][1][0], wf[31], xt[0][1][1][0], Z4); SB0();
    ZC(2, 0, 1, 0); SB0();
    MM(aI[2][1][0], wf[30], xt[0][1][1][0], Z4); SB0();
    ZC(2, 0, 1, 1); SB0();
    MM(aI[2][1][0], wf[31], xt[0][1][0][0], aI[2][1][0]); SB0();
    ZC(2, 0, 1, 2); SB0();
    SB0();
    WAITV(7, wf[32], wf[33]); SB0();
    MM(aP[2][1][0], wf[32], xt[1][1][0][0], aP[2][1][0]); SB0();
    ZC(2, 0, 1, 3); SB0();
    ZW(2, 0, 1); SB0();
    MM(aQ[2][1][0], wf[33], xt[1][1][1][0], aQ[2][1][0]); SB0();
    MM(aI[2][1][0], wf[32], xt[1][1][1][0], aI[2][1][0]); SB0();
    MM(aI[2][1][0], wf[33], xt[1][1][0][0], aI[2][1][0]); SB0();
    WAITV(3, wf[34], wf[35]); SB0();
    MM(aP[2][1][0], wf[34], xt[2][1][0][0], aP[2][1][0]); SB0();
    MM(aQ[2][1][0], wf[35], xt[2][1][1][0], aQ[2][1][0]); SB0();
    MM(aI[2][1][0], wf[34], xt[2][1][1][0], aI[2][1][0]); SB0();
    MM(aI[2][1][0], wf[35], xt[2][1][0][0], aI[2][1][0]); SB0();
    STAMP(5);
    ZC(2, 1, 0, 0); SB0();
    ZC(2, 1, 0, 1); SB0();
    ZC(2, 1, 0, 2); SB0();
    ZC(2, 1, 0, 3); SB0();
    ZW(2, 1, 0); SB0();
    VMW(0); ARRY(1); SB0();
    STAMP(1);
    SB0();
    STAMP(2);
    LGW0(); BAR(); SB0();
    }
  }
  STAMP(6);
  ZRD(0, 2 * M_Z1); ZRD(1, 2 * M_Z1 + 2 * M_ZT);
  SB0();
  asm volatile("" : "+v"(vL));
  const unsigned z0 = min(wave * 16u + (vL >> 5) * 128u, 256u) + (vL & 16u) / 2;
  POLLM(z0, flc, 0x3full, " sc1");
  lane = vL >> 4; g = lane >> 4; r = lane & 15; q = (lane >> 2) & 3; p = lane & 3;
  ZINV2(2);
  if (L >= I_NWG) { STAMP(4); VMW(0); STAMP(3); ARRY(2); STAMP_END(11); return; }
  TABG();
  unsigned long long miss;
  asm volatile("s_mov_b64 exec, 0x3f\n\ts_waitcnt vmcnt(2)\n\tv_cmp_ne_u64 %0, %2, %1\n\ts_mov_b64 exec, -1\n\ts_nop 3" : "=s"(miss), "+v"(pv) : "s"(tag) : "memory");
  if (__builtin_expect(miss == 0, 1)) { IDMA(0); VMW(2); ARRY(2); POLL(1); STAMP(3); VMW(1); }
  else { VMW(0); ARRY(2); STAMP(3); CHECKM(1, z0, flc, 0x3full, POLL_SLEEP); IDMA(0); POLL(1); VMW(0); asm volatile("fftconv_code_end:"); }
  asm volatile("s_waitcnt lgkmcnt(0)" : "+v"(gi[0][0]), "+v"(gi[0][1]), "+v"(gi[1][0]), "+v"(gi[1][1]) :: "memory");
  if (wave == 7) CHECK(0, 1);
  const int ifw = 2 * g + (q >> 1);
  const unsigned ia = sbase + M_RA + (p >> 1) * 512 + (q & 1) * 32 + (p & 1) * 16;
  const unsigned i0 = ia + ifw * 1024 + ((wave ^ (ifw & 3)) << 6), is = ia + 16 * 1024 + (wave << 6);
  const rsrc_t rs = make_rsrc(outp, IMG * IMG * CH * 2);

  const unsigned vo = (r < 14) ? (unsigned)((px0 + r) * (CH * 2) + g * 16) : OOB;
  constexpr unsigned C14H = split_hl(1.0 / 14.0, 0), C14L = split_hl(1.0 / 14.0, 1);
  const u32x4 se[2] = {u32x4{0u, 0u, (lane == 0) ? C14H : 0u, (lane == 0) ? C14L : 0u}, u32x4{0u, 0u, (lane == 13) ? C14H << 16 : 0u, (lane == 13) ? C14L << 16 : 0u}};
  u32x2 id[2][2][3];
  u32x4 io[2][2];
#define IRD(s) do { _Pragma("unroll") for (int t_ = 0; t_ < 2; ++t_) { \
    TRR(id[(s) & 1][t_][0], i0, (s) * YS + t_ * 8); TRR(id[(s) & 1][t_][1], i0, (s) * YS + t_ * 8 + 8192); TRR(id[(s) & 1][t_][2], is, (s) * YS + t_ * 8); } } while (0)
#define IINV(s, n) do { u32x2 (&d_)[2][3] = id[(s) & 1]; \
    asm volatile("s_waitcnt lgkmcnt(%0)" :: "i"(n) : "memory"); SB0(); \
    u32x6 v_[2] = {u32x6{d_[0][0][0], d_[0][0][1], d_[0][1][0], d_[0][1][1], d_[0][2][0], d_[0][2][1]}, \
                   u32x6{d_[1][0][0], d_[1][0][1], d_[1][1][0], d_[1][1][1], d_[1][2][0], d_[1][2][1]}}; \
    asm volatile("" : "+v"(v_[0]), "+v"(v_[1])); \
    _Pragma("unroll") for (int pt = 0; pt < 2; ++pt) { f32x4 e_[2]; \
      _Pragma("unroll") for (int t_ = 0; t_ < 2; ++t_) { const u32x4 a_ = __builtin_shufflevector(v_[t_], v_[t_], 0, 1, 2, 3); \
        e_[t_] = mfma32(a_, gi[pt][0], mfma32(a_, gi[pt][1], mfma32(__builtin_shufflevector(v_[t_], v_[t_], 2, 3, 4, 5), se[pt], Z4))); } \
      io[(s) & 1][pt] = pack8(e_[0], e_[1]); } } while (0)
#define IST(s) do { __builtin_amdgcn_raw_buffer_store_b128(io[(s) & 1][0], rs, vo + (s) * 64, 0, ST_AUX); \
    __builtin_amdgcn_raw_buffer_store_b128(io[(s) & 1][1], rs, vo + (s) * 64 + 14 * CH * 2, 0, ST_AUX); } while (0)
  STAMP(7);
  BAR();
  IDMA(1); POLL(2);
  IRD(0); IINV(0, 0);
  if (wave < NEW) { IST(0); VMW(2); }
  else CHECK(0, 2);
  STAMP(8);
  BAR();
  IDMA(2);
  if (wave < NEW) { IRD(1); IINV(1, 0); IST(1); VMW(2); }
  else { IST(0); IRD(1); IINV(1, 0); IST(1); VMW(4); }
  STAMP(9);
  BAR();
  IRD(2);
  IINV(2, 0); IST(2);
  STAMP_END(11);
}

extern "C" void kernel_launch(void* const* d_in, const int* in_sizes, int n_in,
                              void* d_out, int out_size, void* d_ws, size_t ws_size,
                              hipStream_t stream) {
  const char* x = (const char*)d_in[0];
  const char* w = (const char*)d_in[1];
  char* ws = (char*)d_ws;
  k_conv<<<dim3(F_NWG), dim3(512), 0, stream>>>(x, w, ws, (char*)d_out INSTR_ARG(0));
}
```
